```python
import torch

def _softmax(x, axis=-1):
    return torch.softmax(x, dim=axis)
def _gelu(x):
    return torch.nn.functional.gelu(x, approximate="tanh")
def _silu(x):
    return torch.nn.functional.silu(x)
def _sigmoid(x):
    return torch.sigmoid(x)
def _layernorm(x, axis=-1, eps=1e-5):
    m = x.mean(dim=axis, keepdim=True)
    v = x.var(dim=axis, keepdim=True, unbiased=False)
    return (x - m) / torch.sqrt(v + eps)
def _scatter_add(tgt, idx, src, axis=0):
    return tgt.clone().index_add_(axis, idx, src)
def _segment_sum(src, seg, n):
    out = torch.zeros((n,) + src.shape[1:], dtype=src.dtype)
    return out.index_add_(0, seg, src)
def _pad(x, pw):
    flat = tuple(v for pair in reversed(pw) for v in pair)
    return torch.nn.functional.pad(x, flat)

kernel_name = 'standalone__gqa_block_cached__near_ridge'

def setup_inputs():

    _w = (lambda a: (lambda t: t.to(torch.bfloat16) if t.is_floating_point() else t)(torch.as_tensor(a)))
    B, Sq, D = 8, 112, 512
    Hq, Hkv, Sk, Dh = 32, 32, 1024, 128
    assert Hq % Hkv == 0, (Hq, Hkv)
    return {
        "x": _w((lambda *s: torch.randn(*s, dtype=torch.bfloat16) * 0.8)(B, Sq, D)),
        "k_cache": _w((lambda *s: torch.randn(*s, dtype=torch.bfloat16) * 0.8)(B, Hkv, Sk, Dh)),
        "v_cache": _w((lambda *s: torch.randn(*s, dtype=torch.bfloat16) * 0.05)(B, Hkv, Sk, Dh)),
        "wq": _w((lambda *s: torch.randn(*s, dtype=torch.bfloat16) * 0.8)(D, Hq * Dh) * (D ** -0.5)),
        "wo": _w((lambda *s: torch.randn(*s, dtype=torch.bfloat16) * 0.05)(Hq * Dh, D) * ((Hq * Dh) ** -0.5)),
    }

def reference(x, k_cache, v_cache, wq, wo):
    B, Sq, D = x.shape
    Hq, Dh = 32, 128
    Hkv = k_cache.shape[1]
    rep = Hq // Hkv
    q = (lambda x, *d: x.permute(*d))((x @ wq).reshape(B, Sq, Hq, Dh), 0, 2, 1, 3)
    k = (lambda x, n, axis: torch.repeat_interleave(x, n, dim=axis))(k_cache, rep, axis=1)
    v = (lambda x, n, axis: torch.repeat_interleave(x, n, dim=axis))(v_cache, rep, axis=1)
    scores = torch.einsum("bhqd,bhkd->bhqk", q, k) * (Dh ** -0.5)
    p = _softmax(scores, axis=-1)
    o = torch.einsum("bhqk,bhkd->bhqd", p, v)
    return (lambda x, *d: x.permute(*d))(o, 0, 2, 1, 3).reshape(B, Sq, Hq * Dh) @ wo
```

```cpp
#include <hip/hip_runtime.h>
#include <hip/hip_bf16.h>
#include <stdint.h>

typedef short bf16x8 __attribute__((ext_vector_type(8)));
typedef short s16x4 __attribute__((ext_vector_type(4)));
typedef float f32x16 __attribute__((ext_vector_type(16)));
typedef float f32x4 __attribute__((ext_vector_type(4)));
typedef float f32x2 __attribute__((ext_vector_type(2)));
typedef unsigned u32x4 __attribute__((ext_vector_type(4)));
typedef unsigned u32x2 __attribute__((ext_vector_type(2)));
typedef __bf16 bf16x2_t __attribute__((ext_vector_type(2)));

constexpr int NB = 8, SQ = 112, DM = 512, NH = 32, SK = 1024, DH = 128;
constexpr int HD = NH * DH;

#define LDS_AS __attribute__((address_space(3)))

__device__ __forceinline__ unsigned pk2(float a, float b) {
  f32x2 v = {a, b};
  bf16x2_t r = __builtin_convertvector(v, bf16x2_t);
  return __builtin_bit_cast(unsigned, r);
}

__device__ __forceinline__ unsigned fsw(unsigned row) { return ((row & 3) << 2) | ((row >> 2) & 3); }

__device__ __forceinline__ s16x4 tr_read(unsigned lds_byte_addr) {
  LDS_AS s16x4* p = (LDS_AS s16x4*)(uintptr_t)lds_byte_addr;
  return __builtin_amdgcn_ds_read_tr16_b64_v4i16(p);
}
__device__ __forceinline__ bf16x8 lds_read16(unsigned lds_byte_addr) {
  LDS_AS bf16x8* p = (LDS_AS bf16x8*)(uintptr_t)lds_byte_addr;
  return *p;
}

constexpr unsigned W_OFF = 0;
constexpr unsigned KV_OFF = 65536;
constexpr unsigned LDS_BYTES = 163840;

__global__ __launch_bounds__(256, 1) void attn_kernel(const short* __restrict__ x, const short* __restrict__ kc,
                                                      const short* __restrict__ vc, const short* __restrict__ wq,
                                                      short* __restrict__ ows) {
  extern __shared__ __attribute__((aligned(16))) char smem[];
  const unsigned lds0 = (unsigned)(uintptr_t)smem;
  const int tid = threadIdx.x;
  const int w = __builtin_amdgcn_readfirstlane(tid >> 6);
  const int lane = tid & 63;
  const int r = lane & 31, hi = lane >> 5;
  const int bid = blockIdx.x;
  const int h = (bid & 7) * 4 + ((bid >> 3) & 3);
  const int b = bid >> 5;

  const short* Kh = kc + ((size_t)(b * NH + h)) * SK * DH;
  const short* Vh = vc + ((size_t)(b * NH + h)) * SK * DH;
  const short* Wh = wq + h * DH;

  const unsigned lrow = lane >> 4;
  auto dma_tile = [&](const short* gbase, unsigned ld_bytes, unsigned lds_tile) {
#pragma unroll
    for (int j = 0; j < 4; ++j) {
      unsigned row = 16 * w + 4 * j + lrow;
      unsigned ch = (lane & 15) ^ ((lrow << 2) | j);
      const char* src = (const char*)gbase + (size_t)row * ld_bytes + ch * 16;
      __builtin_amdgcn_global_load_lds((const __attribute__((address_space(1))) void*)src,
                                       (LDS_AS void*)(uintptr_t)(lds_tile + (16 * w + 4 * j) * 256), 16, 0, 0);
    }
  };

  int qrow = 32 * w + r;
  int qc = qrow < SQ ? qrow : SQ - 1;
  const short* xr = x + ((size_t)(b * SQ + qc)) * DM + 8 * hi;
  bf16x8 xf[32];
#pragma unroll
  for (int ks = 0; ks < 32; ++ks) xf[ks] = *(const bf16x8*)(xr + 16 * ks);

  dma_tile(Wh + (size_t)0 * 64 * HD, HD * 2, lds0 + W_OFF + 0 * 16384);
  dma_tile(Wh + (size_t)1 * 64 * HD, HD * 2, lds0 + W_OFF + 1 * 16384);
  dma_tile(Wh + (size_t)2 * 64 * HD, HD * 2, lds0 + W_OFF + 2 * 16384);
  dma_tile(Kh + 0 * 64 * DH, 256, lds0 + KV_OFF + 0 * 32768);
  dma_tile(Vh + 0 * 64 * DH, 256, lds0 + KV_OFF + 0 * 32768 + 16384);
  dma_tile(Kh + 1 * 64 * DH, 256, lds0 + KV_OFF + 1 * 32768);
  dma_tile(Vh + 1 * 64 * DH, 256, lds0 + KV_OFF + 1 * 32768 + 16384);

  const unsigned blk = (lane >> 4) & 1, q4 = (lane & 15) >> 2, p = lane & 3;
  const unsigned lo2 = 2 * blk + (p >> 1);

  f32x16 qacc[4];
#pragma unroll
  for (int c = 0; c < 4; ++c) qacc[c] = f32x16{};
  {
    unsigned wa[4][2];
#pragma unroll
    for (int c = 0; c < 4; ++c)
#pragma unroll
      for (int t = 0; t < 2; ++t)
        wa[c][t] = 256 * (8 * hi + 4 * t + q4) + 16 * (4 * (c ^ q4) + (lo2 ^ (2 * hi + t))) + 8 * (p & 1);
#pragma unroll
    for (int t8 = 0; t8 < 8; ++t8) {
      if (t8 < 3) asm volatile("s_waitcnt vmcnt(24)" ::: "memory");
      else if (t8 < 6) asm volatile("s_waitcnt vmcnt(8)" ::: "memory");
      else if (t8 == 6) asm volatile("s_waitcnt vmcnt(4)" ::: "memory");
      else asm volatile("s_waitcnt vmcnt(0)" ::: "memory");
      __builtin_amdgcn_s_barrier();
      if (t8 + 3 < 8) dma_tile(Wh + (size_t)(t8 + 3) * 64 * HD, HD * 2, lds0 + W_OFF + ((t8 + 3) & 3) * 16384);
      unsigned base = lds0 + W_OFF + (t8 & 3) * 16384;
#pragma unroll
      for (int ks = 0; ks < 4; ++ks) {
#pragma unroll
        for (int c = 0; c < 4; ++c) {
          s16x4 a0 = tr_read(base + wa[c][0] + ks * 4096);
          s16x4 a1 = tr_read(base + wa[c][1] + ks * 4096);
          bf16x8 a = {a0[0], a0[1], a0[2], a0[3], a1[0], a1[1], a1[2], a1[3]};
          qacc[c] = __builtin_amdgcn_mfma_f32_32x32x16_bf16(a, xf[t8 * 4 + ks], qacc[c], 0, 0, 0);
        }
      }
    }
  }
  bf16x8 qf[8];
  {
    unsigned qs = lds0 + W_OFF + w * 8192;
#pragma unroll
    for (int c = 0; c < 4; ++c)
#pragma unroll
      for (int g = 0; g < 4; ++g) {
        u32x2 v = {pk2(qacc[c][4 * g + 0], qacc[c][4 * g + 1]), pk2(qacc[c][4 * g + 2], qacc[c][4 * g + 3])};
        unsigned ch = 4 * c + g;
        unsigned a = qs + r * 256 + 16 * (ch ^ (r & 15)) + 8 * hi;
        *(LDS_AS u32x2*)(uintptr_t)a = v;
      }
    asm volatile("s_waitcnt lgkmcnt(0)" ::: "memory");
#pragma unroll
    for (int s = 0; s < 8; ++s) {
      unsigned ch = 2 * s + hi;
      qf[s] = lds_read16(qs + r * 256 + 16 * (ch ^ (r & 15)));
    }
  }

  const float cs = 0.08838834764831845f * 1.4426950408889634f;
  float m = -__builtin_inff(), l = 0.f;
  f32x16 oacc[4];
#pragma unroll
  for (int c = 0; c < 4; ++c) oacc[c] = f32x16{};
  unsigned ka[8];
#pragma unroll
  for (int s = 0; s < 8; ++s) ka[s] = 256 * r + 16 * ((2 * s + hi) ^ fsw(r));
  unsigned va[4][2];
#pragma unroll
  for (int c = 0; c < 4; ++c)
#pragma unroll
    for (int t = 0; t < 2; ++t)
      va[c][t] = 16384 + 256 * (8 * t + 4 * hi + q4) + 16 * (4 * (c ^ q4) + (lo2 ^ (2 * t + hi))) + 8 * (p & 1);

  unsigned slot = 0;
  for (int i = 0; i < 16; ++i) {
    if (i < 15) asm volatile("s_waitcnt vmcnt(8)" ::: "memory");
    else asm volatile("s_waitcnt vmcnt(0)" ::: "memory");
    __builtin_amdgcn_s_barrier();
    if (i + 2 < 16) {
      unsigned ns = slot + 2; if (ns >= 3) ns -= 3;
      dma_tile(Kh + (size_t)(i + 2) * 64 * DH, 256, lds0 + KV_OFF + ns * 32768);
      dma_tile(Vh + (size_t)(i + 2) * 64 * DH, 256, lds0 + KV_OFF + ns * 32768 + 16384);
    }
    unsigned base = lds0 + KV_OFF + slot * 32768;
    f32x16 s0 = f32x16{}, s1 = f32x16{};
#pragma unroll
    for (int s = 0; s < 8; ++s) {
      bf16x8 k0 = lds_read16(base + ka[s]);
      bf16x8 k1 = lds_read16(base + ka[s] + 8192);
      s0 = __builtin_amdgcn_mfma_f32_32x32x16_bf16(k0, qf[s], s0, 0, 0, 0);
      s1 = __builtin_amdgcn_mfma_f32_32x32x16_bf16(k1, qf[s], s1, 0, 0, 0);
    }
    float mx = s0[0];
#pragma unroll
    for (int j = 1; j < 16; ++j) mx = fmaxf(mx, s0[j]);
#pragma unroll
    for (int j = 0; j < 16; ++j) mx = fmaxf(mx, s1[j]);
    {
      auto rr = __builtin_amdgcn_permlane32_swap(__float_as_uint(mx), __float_as_uint(mx), false, false);
      mx = fmaxf(__uint_as_float(rr[0]), __uint_as_float(rr[1]));
    }
    float mnew = fmaxf(m, mx * cs);
    float alpha = __builtin_amdgcn_exp2f(m - mnew);
    m = mnew;
    float ps = 0.f;
#pragma unroll
    for (int j = 0; j < 16; ++j) { s0[j] = __builtin_amdgcn_exp2f(fmaf(s0[j], cs, -mnew)); ps += s0[j]; }
#pragma unroll
    for (int j = 0; j < 16; ++j) { s1[j] = __builtin_amdgcn_exp2f(fmaf(s1[j], cs, -mnew)); ps += s1[j]; }
    l = l * alpha + ps;
    if (__any(alpha != 1.f)) {
#pragma unroll
      for (int c = 0; c < 4; ++c) oacc[c] *= alpha;
    }
    bf16x8 pb[4];
#pragma unroll
    for (int sp = 0; sp < 2; ++sp) {
      u32x4 u0 = {pk2(s0[8 * sp + 0], s0[8 * sp + 1]), pk2(s0[8 * sp + 2], s0[8 * sp + 3]),
                  pk2(s0[8 * sp + 4], s0[8 * sp + 5]), pk2(s0[8 * sp + 6], s0[8 * sp + 7])};
      u32x4 u1 = {pk2(s1[8 * sp + 0], s1[8 * sp + 1]), pk2(s1[8 * sp + 2], s1[8 * sp + 3]),
                  pk2(s1[8 * sp + 4], s1[8 * sp + 5]), pk2(s1[8 * sp + 6], s1[8 * sp + 7])};
      pb[sp] = __builtin_bit_cast(bf16x8, u0);
      pb[2 + sp] = __builtin_bit_cast(bf16x8, u1);
    }
#pragma unroll
    for (int ks = 0; ks < 4; ++ks) {
#pragma unroll
      for (int c = 0; c < 4; ++c) {
        s16x4 a0 = tr_read(base + va[c][0] + ks * 4096);
        s16x4 a1 = tr_read(base + va[c][1] + ks * 4096);
        bf16x8 a = {a0[0], a0[1], a0[2], a0[3], a1[0], a1[1], a1[2], a1[3]};
        oacc[c] = __builtin_amdgcn_mfma_f32_32x32x16_bf16(a, pb[ks], oacc[c], 0, 0, 0);
      }
    }
    slot = slot + 1; if (slot >= 3) slot = 0;
  }
  {
    auto rr = __builtin_amdgcn_permlane32_swap(__float_as_uint(l), __float_as_uint(l), false, false);
    float lt = __uint_as_float(rr[0]) + __uint_as_float(rr[1]);
    float inv = 1.f / lt;
    if (qrow < SQ) {
      short* orow = ows + ((size_t)(b * SQ + qrow)) * HD + h * DH + 4 * hi;
#pragma unroll
      for (int c = 0; c < 4; ++c)
#pragma unroll
        for (int g = 0; g < 4; ++g) {
          u32x2 v = {pk2(oacc[c][4 * g + 0] * inv, oacc[c][4 * g + 1] * inv),
                     pk2(oacc[c][4 * g + 2] * inv, oacc[c][4 * g + 3] * inv)};
          *(u32x2*)(orow + 32 * c + 8 * g) = v;
        }
    }
  }
}

constexpr int G2_BK = 128;
__global__ __launch_bounds__(256, 2) void oproj_kernel(const short* __restrict__ O, const short* __restrict__ wo,
                                                       short* __restrict__ out) {
  __shared__ __attribute__((aligned(16))) char smem[2 * G2_BK * 64];
  const unsigned lds0 = (unsigned)(uintptr_t)smem;
  const int tid = threadIdx.x;
  const int w = __builtin_amdgcn_readfirstlane(tid >> 6);
  const int lane = tid & 63;
  const int mt = blockIdx.x >> 4, nt = blockIdx.x & 15;
  const int m0 = mt * 64, n0 = nt * 32;
  const int g = lane >> 4, li = lane & 15, q4 = li >> 2, p = lane & 3;
  const short* Arow = O + (size_t)(m0 + 16 * w + li) * HD + 8 * g;
  auto stageB = [&](int k0, int buf) {
#pragma unroll
    for (int j = 0; j < 2; ++j) {
      int ins = 2 * w + j;
      int row = 16 * ins + (lane >> 2);
      int ch = (lane & 3) ^ ((row >> 3) & 1) * 2;
      const short* src = wo + (size_t)(k0 + row) * 512 + n0 + ch * 8;
      __builtin_amdgcn_global_load_lds((const __attribute__((address_space(1))) void*)src,
                                       (LDS_AS void*)(uintptr_t)(lds0 + buf * 8192 + ins * 1024), 16, 0, 0);
    }
  };
  f32x4 acc[2] = {f32x4{}, f32x4{}};
  stageB(0, 0);
  for (int kt = 0; kt < HD / G2_BK; ++kt) {
    asm volatile("s_waitcnt vmcnt(0)" ::: "memory");
    __builtin_amdgcn_s_barrier();
    if (kt + 1 < HD / G2_BK) stageB((kt + 1) * G2_BK, (kt + 1) & 1);
    unsigned base = lds0 + (kt & 1) * 8192;
#pragma unroll
    for (int ks = 0; ks < 4; ++ks) {
      bf16x8 a = *(const bf16x8*)(Arow + kt * G2_BK + ks * 32);
#pragma unroll
      for (int c = 0; c < 2; ++c) {
        s16x4 b0, b1;
        {
          int row0 = 32 * ks + 8 * g + q4;
          int sw = (g & 1) * 32;
          b0 = tr_read(base + row0 * 64 + ((c * 32) ^ sw) + p * 8);
          b1 = tr_read(base + (row0 + 4) * 64 + ((c * 32) ^ sw) + p * 8);
        }
        bf16x8 bb = {b0[0], b0[1], b0[2], b0[3], b1[0], b1[1], b1[2], b1[3]};
        acc[c] = __builtin_amdgcn_mfma_f32_16x16x32_bf16(a, bb, acc[c], 0, 0, 0);
      }
    }
    __builtin_amdgcn_s_barrier();
  }
#pragma unroll
  for (int c = 0; c < 2; ++c)
#pragma unroll
    for (int j = 0; j < 4; ++j) {
      unsigned v = pk2(acc[c][j], 0.f);
      out[(size_t)(m0 + 16 * w + 4 * g + j) * 512 + n0 + 16 * c + li] = (short)(v & 0xffff);
    }
}

extern "C" void kernel_launch(void* const* d_in, const int* in_sizes, int n_in, void* d_out, int out_size,
                              void* d_ws, size_t ws_size, hipStream_t stream) {
  const short* x = (const short*)d_in[0];
  const short* kc = (const short*)d_in[1];
  const short* vc = (const short*)d_in[2];
  const short* wq = (const short*)d_in[3];
  const short* wo = (const short*)d_in[4];
  short* ows = (short*)d_ws;
  hipFuncSetAttribute((const void*)attn_kernel, hipFuncAttributeMaxDynamicSharedMemorySize, LDS_BYTES);
  attn_kernel<<<256, 256, LDS_BYTES, stream>>>(x, kc, vc, wq, ows);
  oproj_kernel<<<224, 256, 0, stream>>>(ows, wo, (short*)d_out);
}
```

```cpp
#include <hip/hip_runtime.h>
#include <hip/hip_bf16.h>
#include <stdint.h>
#include <type_traits>

typedef short bf16x8 __attribute__((ext_vector_type(8)));
typedef short s16x4 __attribute__((ext_vector_type(4)));
typedef float f32x16 __attribute__((ext_vector_type(16)));
typedef float f32x4 __attribute__((ext_vector_type(4)));
typedef float f32x2 __attribute__((ext_vector_type(2)));
typedef unsigned u32x4 __attribute__((ext_vector_type(4)));
typedef unsigned u32x2 __attribute__((ext_vector_type(2)));
typedef __bf16 bf16x2_t __attribute__((ext_vector_type(2)));

constexpr int NB = 8, SQ = 112, DM = 512, NH = 32, SK = 1024, DH = 128;
constexpr int HD = NH * DH;

#define LDS_AS __attribute__((address_space(3)))

__device__ __forceinline__ unsigned pk2(float a, float b) {
  f32x2 v = {a, b};
  bf16x2_t r = __builtin_convertvector(v, bf16x2_t);
  return __builtin_bit_cast(unsigned, r);
}

__device__ __forceinline__ unsigned fsw(unsigned row) { return ((row & 3) << 2) | ((row >> 2) & 3); }

__device__ __forceinline__ s16x4 tr_read(unsigned lds_byte_addr) {
  LDS_AS s16x4* p = (LDS_AS s16x4*)(uintptr_t)lds_byte_addr;
  return __builtin_amdgcn_ds_read_tr16_b64_v4i16(p);
}
__device__ __forceinline__ bf16x8 lds_read16(unsigned lds_byte_addr) {
  LDS_AS bf16x8* p = (LDS_AS bf16x8*)(uintptr_t)lds_byte_addr;
  return *p;
}

template <bool NT>
__device__ __forceinline__ void glds16(const void* gsrc, unsigned lds_dst_uniform) {
  unsigned keep;
  unsigned dst = __builtin_amdgcn_readfirstlane(lds_dst_uniform);
  if constexpr (NT)
    asm volatile("s_mov_b32 %0, m0\n\ts_mov_b32 m0, %2\n\ts_nop 0\n\tglobal_load_lds_dwordx4 %1, off nt\n\ts_mov_b32 m0, %0"
                 : "=&s"(keep) : "v"(gsrc), "s"(dst) : "memory");
  else
    asm volatile("s_mov_b32 %0, m0\n\ts_mov_b32 m0, %2\n\ts_nop 0\n\tglobal_load_lds_dwordx4 %1, off\n\ts_mov_b32 m0, %0"
                 : "=&s"(keep) : "v"(gsrc), "s"(dst) : "memory");
}

#define WAITV(n) asm volatile("s_waitcnt vmcnt(" #n ")" ::: "memory")

constexpr unsigned LDS_BYTES = 163840;

__global__ __launch_bounds__(256, 1) void attn_kernel(const short* __restrict__ x, const short* __restrict__ kc,
                                                      const short* __restrict__ vc, const short* __restrict__ wq,
                                                      const short* __restrict__ wo, short* __restrict__ OT,
                                                      short* __restrict__ WT) {
  extern __shared__ __attribute__((aligned(16))) char smem[];
  const unsigned lds0 = (unsigned)(uintptr_t)smem;
  const int tid = threadIdx.x;
  const int w = __builtin_amdgcn_readfirstlane(tid >> 6);
  const int lane = tid & 63;
  const int r = lane & 31, hi = lane >> 5;
  const int bid = blockIdx.x;
  const int h = (bid & 7) * 4 + ((bid >> 3) & 3);
  const int b = bid >> 5;

  const short* Kh = kc + ((size_t)(b * NH + h)) * SK * DH;
  const short* Vh = vc + ((size_t)(b * NH + h)) * SK * DH;
  const short* Wh = wq + h * DH;

  const unsigned lrow = lane >> 4;
  unsigned so_kv[4], so_w[4];
#pragma unroll
  for (int j = 0; j < 4; ++j) {
    unsigned row = 16 * w + 4 * j + lrow;
    unsigned ch = (lane & 15) ^ ((lrow << 2) | j);
    so_kv[j] = row * 256 + ch * 16;
    so_w[j] = row * (HD * 2) + ch * 16;
  }
  auto dma_item = [&](int n) {
    unsigned slot = (unsigned)n % 10u;
    unsigned dst = lds0 + slot * 16384 + w * 4096;
    if (n < 8) {
      const char* g = (const char*)Wh + (size_t)n * 64 * (HD * 2);
#pragma unroll
      for (int j = 0; j < 4; ++j) glds16<false>(g + so_w[j], dst + j * 1024);
    } else {
      int t = (n - 8) >> 1;
      const char* g = (const char*)((n & 1) ? Vh : Kh) + (size_t)t * 16384;
#pragma unroll
      for (int j = 0; j < 4; ++j) glds16<true>(g + so_kv[j], dst + j * 1024);
    }
  };

  int qrow = 32 * w + r;
  int qc = qrow < SQ ? qrow : SQ - 1;
  const short* xr = x + ((size_t)(b * SQ + qc)) * DM + 8 * hi;
  bf16x8 xf[32];
#pragma unroll
  for (int ks = 0; ks < 32; ++ks)
    asm volatile("global_load_dwordx4 %0, %1, off offset:%2" : "=v"(xf[ks]) : "v"(xr), "i"(32 * ks) : "memory");
  unsigned wv[32];
  {
    const short* src = wo + (size_t)((bid >> 1) * 32) * 512 + (bid & 1) * 256 + tid;
#pragma unroll
    for (int kk = 0; kk < 32; ++kk)
      asm volatile("global_load_ushort %0, %1, off offset:%2" : "=v"(wv[kk]) : "v"(src + (kk >> 2) * 2048), "i"((kk & 3) * 1024) : "memory");
  }

#pragma unroll
  for (int n = 0; n < 10; ++n) dma_item(n);

  asm volatile("s_waitcnt vmcnt(40)"
               : "+v"(xf[0]), "+v"(xf[1]), "+v"(xf[2]), "+v"(xf[3]), "+v"(xf[4]), "+v"(xf[5]), "+v"(xf[6]), "+v"(xf[7]),
                 "+v"(xf[8]), "+v"(xf[9]), "+v"(xf[10]), "+v"(xf[11]), "+v"(xf[12]), "+v"(xf[13]), "+v"(xf[14]), "+v"(xf[15])
               :: "memory");
  asm volatile(""
               : "+v"(xf[16]), "+v"(xf[17]), "+v"(xf[18]), "+v"(xf[19]), "+v"(xf[20]), "+v"(xf[21]), "+v"(xf[22]), "+v"(xf[23]),
                 "+v"(xf[24]), "+v"(xf[25]), "+v"(xf[26]), "+v"(xf[27]), "+v"(xf[28]), "+v"(xf[29]), "+v"(xf[30]), "+v"(xf[31])
               :: "memory");
  asm volatile(""
               : "+v"(wv[0]), "+v"(wv[1]), "+v"(wv[2]), "+v"(wv[3]), "+v"(wv[4]), "+v"(wv[5]), "+v"(wv[6]), "+v"(wv[7]),
                 "+v"(wv[8]), "+v"(wv[9]), "+v"(wv[10]), "+v"(wv[11]), "+v"(wv[12]), "+v"(wv[13]), "+v"(wv[14]), "+v"(wv[15])
               :: "memory");
  asm volatile(""
               : "+v"(wv[16]), "+v"(wv[17]), "+v"(wv[18]), "+v"(wv[19]), "+v"(wv[20]), "+v"(wv[21]), "+v"(wv[22]), "+v"(wv[23]),
                 "+v"(wv[24]), "+v"(wv[25]), "+v"(wv[26]), "+v"(wv[27]), "+v"(wv[28]), "+v"(wv[29]), "+v"(wv[30]), "+v"(wv[31])
               :: "memory");
  {
    const int kb = bid >> 1, nh = bid & 1;
    char* dst = (char*)WT + ((size_t)((nh * 16 + (tid >> 4)) * 128 + kb)) * 1024 + (tid & 15) * 16;
#pragma unroll
    for (int g = 0; g < 4; ++g) {
      u32x4 o = {wv[8 * g + 0] | (wv[8 * g + 1] << 16), wv[8 * g + 2] | (wv[8 * g + 3] << 16),
                 wv[8 * g + 4] | (wv[8 * g + 5] << 16), wv[8 * g + 6] | (wv[8 * g + 7] << 16)};
      *(u32x4*)(dst + g * 256) = o;
    }
  }

  const unsigned blk = (lane >> 4) & 1, q4 = (lane & 15) >> 2, p = lane & 3;
  const unsigned lo2 = 2 * blk + (p >> 1);

  f32x16 qacc[4];
#pragma unroll
  for (int c = 0; c < 4; ++c) qacc[c] = f32x16{};
  {
    unsigned wa[4][2];
#pragma unroll
    for (int c = 0; c < 4; ++c)
#pragma unroll
      for (int t = 0; t < 2; ++t)
        wa[c][t] = 256 * (8 * hi + 4 * t + q4) + 16 * (4 * (c ^ q4) + (lo2 ^ (2 * hi + t))) + 8 * (p & 1);
#pragma unroll
    for (int t8 = 0; t8 < 8; ++t8) {
      if (t8 <= 6) WAITV(36);
      else WAITV(32);
      __builtin_amdgcn_s_barrier();
      if (t8 >= 1 && t8 <= 5) dma_item(t8 + 9);
      unsigned base = lds0 + t8 * 16384;
      s16x4 af[4][4][2];
#pragma unroll
      for (int ks = 0; ks < 4; ++ks)
#pragma unroll
        for (int c = 0; c < 4; ++c) {
          af[ks][c][0] = tr_read(base + wa[c][0] + ks * 4096);
          af[ks][c][1] = tr_read(base + wa[c][1] + ks * 4096);
        }
#pragma unroll
      for (int ks = 0; ks < 4; ++ks) {
#pragma unroll
        for (int c = 0; c < 4; ++c) {
          s16x4 a0 = af[ks][c][0], a1 = af[ks][c][1];
          bf16x8 a = {a0[0], a0[1], a0[2], a0[3], a1[0], a1[1], a1[2], a1[3]};
          qacc[c] = __builtin_amdgcn_mfma_f32_32x32x16_bf16(a, xf[t8 * 4 + ks], qacc[c], 0, 0, 0);
        }
      }
    }
  }
  bf16x8 qf[8];
  {
    unsigned qs = lds0 + 5 * 16384 + w * 8192;
#pragma unroll
    for (int c = 0; c < 4; ++c)
#pragma unroll
      for (int g = 0; g < 4; ++g) {
        u32x2 v = {pk2(qacc[c][4 * g + 0], qacc[c][4 * g + 1]), pk2(qacc[c][4 * g + 2], qacc[c][4 * g + 3])};
        unsigned ch = 4 * c + g;
        unsigned a = qs + r * 256 + 16 * (ch ^ (r & 15)) + 8 * hi;
        *(LDS_AS u32x2*)(uintptr_t)a = v;
      }
    asm volatile("s_waitcnt lgkmcnt(0)" ::: "memory");
#pragma unroll
    for (int s = 0; s < 8; ++s) {
      unsigned ch = 2 * s + hi;
      qf[s] = lds_read16(qs + r * 256 + 16 * (ch ^ (r & 15)));
    }
    asm volatile("s_waitcnt lgkmcnt(0)" ::: "memory");
  }

  const float cs = 0.08838834764831845f * 1.4426950408889634f;
  float m = -__builtin_inff(), l = 0.f;
  f32x16 oacc[4];
#pragma unroll
  for (int c = 0; c < 4; ++c) oacc[c] = f32x16{};
  unsigned ka[8];
#pragma unroll
  for (int s = 0; s < 8; ++s) ka[s] = 256 * r + 16 * ((2 * s + hi) ^ fsw(r));
  unsigned va[4][2];
#pragma unroll
  for (int c = 0; c < 4; ++c)
#pragma unroll
    for (int t = 0; t < 2; ++t)
      va[c][t] = 256 * (8 * t + 4 * hi + q4) + 16 * (4 * (c ^ q4) + (lo2 ^ (2 * t + hi))) + 8 * (p & 1);

  unsigned kslot = 8;
  for (int i = 0; i < 16; ++i) {
    if (i == 0) WAITV(20);
    else if (i <= 12) WAITV(24);
    else if (i == 13) WAITV(16);
    else if (i == 14) WAITV(8);
    else WAITV(0);
    __builtin_amdgcn_s_barrier();
    if (i == 0) { dma_item(15); dma_item(16); dma_item(17); }
    else if (i <= 11) { dma_item(2 * i + 16); dma_item(2 * i + 17); }
    unsigned vslot = kslot + 1; if (vslot >= 10) vslot -= 10;
    unsigned kbase = lds0 + kslot * 16384, vbase = lds0 + vslot * 16384;
    kslot += 2; if (kslot >= 10) kslot -= 10;
    f32x16 s0 = f32x16{}, s1 = f32x16{};
    {
      bf16x8 kf[8][2];
#pragma unroll
      for (int s = 0; s < 8; ++s) {
        kf[s][0] = lds_read16(kbase + ka[s]);
        kf[s][1] = lds_read16(kbase + ka[s] + 8192);
      }
#pragma unroll
      for (int s = 0; s < 8; ++s) {
        s0 = __builtin_amdgcn_mfma_f32_32x32x16_bf16(kf[s][0], qf[s], s0, 0, 0, 0);
        s1 = __builtin_amdgcn_mfma_f32_32x32x16_bf16(kf[s][1], qf[s], s1, 0, 0, 0);
      }
    }
    s16x4 vf[4][4][2];
#pragma unroll
    for (int ks = 0; ks < 4; ++ks)
#pragma unroll
      for (int c = 0; c < 4; ++c) {
        vf[ks][c][0] = tr_read(vbase + va[c][0] + ks * 4096);
        vf[ks][c][1] = tr_read(vbase + va[c][1] + ks * 4096);
      }
    float mx = s0[0];
#pragma unroll
    for (int j = 1; j < 16; ++j) mx = fmaxf(mx, s0[j]);
#pragma unroll
    for (int j = 0; j < 16; ++j) mx = fmaxf(mx, s1[j]);
    {
      auto rr = __builtin_amdgcn_permlane32_swap(__float_as_uint(mx), __float_as_uint(mx), false, false);
      mx = fmaxf(__uint_as_float(rr[0]), __uint_as_float(rr[1]));
    }
    float mnew = fmaxf(m, mx * cs);
    float alpha = __builtin_amdgcn_exp2f(m - mnew);
    m = mnew;
    float ps = 0.f;
#pragma unroll
    for (int j = 0; j < 16; ++j) { s0[j] = __builtin_amdgcn_exp2f(fmaf(s0[j], cs, -mnew)); ps += s0[j]; }
#pragma unroll
    for (int j = 0; j < 16; ++j) { s1[j] = __builtin_amdgcn_exp2f(fmaf(s1[j], cs, -mnew)); ps += s1[j]; }
    l = l * alpha + ps;
    if (__any(alpha != 1.f)) {
#pragma unroll
      for (int c = 0; c < 4; ++c) oacc[c] *= alpha;
    }
    bf16x8 pb[4];
#pragma unroll
    for (int sp = 0; sp < 2; ++sp) {
      u32x4 u0 = {pk2(s0[8 * sp + 0], s0[8 * sp + 1]), pk2(s0[8 * sp + 2], s0[8 * sp + 3]),
                  pk2(s0[8 * sp + 4], s0[8 * sp + 5]), pk2(s0[8 * sp + 6], s0[8 * sp + 7])};
      u32x4 u1 = {pk2(s1[8 * sp + 0], s1[8 * sp + 1]), pk2(s1[8 * sp + 2], s1[8 * sp + 3]),
                  pk2(s1[8 * sp + 4], s1[8 * sp + 5]), pk2(s1[8 * sp + 6], s1[8 * sp + 7])};
      pb[sp] = __builtin_bit_cast(bf16x8, u0);
      pb[2 + sp] = __builtin_bit_cast(bf16x8, u1);
    }
#pragma unroll
    for (int ks = 0; ks < 4; ++ks) {
#pragma unroll
      for (int c = 0; c < 4; ++c) {
        s16x4 a0 = vf[ks][c][0], a1 = vf[ks][c][1];
        bf16x8 a = {a0[0], a0[1], a0[2], a0[3], a1[0], a1[1], a1[2], a1[3]};
        oacc[c] = __builtin_amdgcn_mfma_f32_32x32x16_bf16(a, pb[ks], oacc[c], 0, 0, 0);
      }
    }
  }
  {
    auto rr = __builtin_amdgcn_permlane32_swap(__float_as_uint(l), __float_as_uint(l), false, false);
    float lt = __uint_as_float(rr[0]) + __uint_as_float(rr[1]);
    float inv = 1.f / lt;
    if (qrow < SQ) {
      char* ob = (char*)OT + ((size_t)((7 * b + (qrow >> 4)) * 128 + 4 * h)) * 1024 + (qrow & 15) * 16 + 8 * hi;
#pragma unroll
      for (int c = 0; c < 4; ++c)
#pragma unroll
        for (int g = 0; g < 4; ++g) {
          u32x2 v = {pk2(oacc[c][4 * g + 0] * inv, oacc[c][4 * g + 1] * inv),
                     pk2(oacc[c][4 * g + 2] * inv, oacc[c][4 * g + 3] * inv)};
          *(u32x2*)(ob + c * 1024 + g * 256) = v;
        }
    }
  }
}

constexpr int PD = 8;
__global__ __launch_bounds__(256, 2) void oproj_kernel(const short* __restrict__ OT, const short* __restrict__ WT,
                                                       short* __restrict__ out) {
  const int tid = threadIdx.x;
  const int w = tid >> 6;
  const int lane = tid & 63;
  const int bid = blockIdx.x;
  const int mt = bid % 14, nt = bid / 14;
  const char* Ab = (const char*)OT + ((size_t)((mt * 4 + w) * 128)) * 1024 + lane * 16;
  const char* Bb0 = (const char*)WT + ((size_t)((nt * 2 + 0) * 128)) * 1024 + lane * 16;
  const char* Bb1 = (const char*)WT + ((size_t)((nt * 2 + 1) * 128)) * 1024 + lane * 16;
  bf16x8 a[PD], b0[PD], b1[PD];
#pragma unroll
  for (int d = 0; d < PD; ++d) {
    a[d] = *(const bf16x8*)(Ab + d * 1024);
    b0[d] = *(const bf16x8*)(Bb0 + d * 1024);
    b1[d] = *(const bf16x8*)(Bb1 + d * 1024);
  }
  f32x4 acc0 = f32x4{}, acc1 = f32x4{};
  for (int kb = 0; kb < 128; kb += PD) {
#pragma unroll
    for (int d = 0; d < PD; ++d) {
      bf16x8 ca = a[d], cb0 = b0[d], cb1 = b1[d];
      if (kb + PD < 128) {
        a[d] = *(const bf16x8*)(Ab + (kb + PD + d) * 1024);
        b0[d] = *(const bf16x8*)(Bb0 + (kb + PD + d) * 1024);
        b1[d] = *(const bf16x8*)(Bb1 + (kb + PD + d) * 1024);
      }
      acc0 = __builtin_amdgcn_mfma_f32_16x16x32_bf16(ca, cb0, acc0, 0, 0, 0);
      acc1 = __builtin_amdgcn_mfma_f32_16x16x32_bf16(ca, cb1, acc1, 0, 0, 0);
    }
  }
  const int g = lane >> 4, li = lane & 15;
  const int m0 = mt * 64 + 16 * w + 4 * g, n0 = nt * 32 + li;
#pragma unroll
  for (int j = 0; j < 4; ++j) {
    unsigned v0 = pk2(acc0[j], 0.f), v1 = pk2(acc1[j], 0.f);
    out[(size_t)(m0 + j) * 512 + n0] = (short)(v0 & 0xffff);
    out[(size_t)(m0 + j) * 512 + n0 + 16] = (short)(v1 & 0xffff);
  }
}

extern "C" void kernel_launch(void* const* d_in, const int* in_sizes, int n_in, void* d_out, int out_size,
                              void* d_ws, size_t ws_size, hipStream_t stream) {
  const short* x = (const short*)d_in[0];
  const short* kc = (const short*)d_in[1];
  const short* vc = (const short*)d_in[2];
  const short* wq = (const short*)d_in[3];
  const short* wo = (const short*)d_in[4];
  short* OT = (short*)d_ws;
  short* WT = (short*)((char*)d_ws + (size_t)8 * 1024 * 1024);
  (void)hipFuncSetAttribute((const void*)attn_kernel, hipFuncAttributeMaxDynamicSharedMemorySize, LDS_BYTES);
  attn_kernel<<<256, 256, LDS_BYTES, stream>>>(x, kc, vc, wq, wo, OT, WT);
  oproj_kernel<<<224, 256, 0, stream>>>(OT, WT, (short*)d_out);
}
```

```cpp
#include <hip/hip_runtime.h>
#include <hip/hip_bf16.h>
#include <stdint.h>
#include <type_traits>

typedef short bf16x8 __attribute__((ext_vector_type(8)));
typedef short s16x4 __attribute__((ext_vector_type(4)));
typedef float f32x16 __attribute__((ext_vector_type(16)));
typedef float f32x4 __attribute__((ext_vector_type(4)));
typedef float f32x2 __attribute__((ext_vector_type(2)));
typedef unsigned u32x4 __attribute__((ext_vector_type(4)));
typedef unsigned u32x2 __attribute__((ext_vector_type(2)));
typedef __bf16 bf16x2_t __attribute__((ext_vector_type(2)));

constexpr int NB = 8, SQ = 112, DM = 512, NH = 32, SK = 1024, DH = 128;
constexpr int HD = NH * DH;

#define LDS_AS __attribute__((address_space(3)))

__device__ __forceinline__ unsigned pk2(float a, float b) {
  f32x2 v = {a, b};
  bf16x2_t r = __builtin_convertvector(v, bf16x2_t);
  return __builtin_bit_cast(unsigned, r);
}

__device__ __forceinline__ unsigned fsw(unsigned row) { return ((row & 3) << 2) | ((row >> 2) & 3); }

__device__ __forceinline__ s16x4 tr_read(unsigned lds_byte_addr) {
  LDS_AS s16x4* p = (LDS_AS s16x4*)(uintptr_t)lds_byte_addr;
  return __builtin_amdgcn_ds_read_tr16_b64_v4i16(p);
}
__device__ __forceinline__ bf16x8 lds_read16(unsigned lds_byte_addr) {
  LDS_AS bf16x8* p = (LDS_AS bf16x8*)(uintptr_t)lds_byte_addr;
  return *p;
}

template <bool NT>
__device__ __forceinline__ void glds16(const void* gsrc, unsigned lds_dst_uniform) {
  unsigned keep;
  unsigned dst = __builtin_amdgcn_readfirstlane(lds_dst_uniform);
  if constexpr (NT)
    asm volatile("s_mov_b32 %0, m0\n\ts_mov_b32 m0, %2\n\ts_nop 0\n\tglobal_load_lds_dwordx4 %1, off nt\n\ts_mov_b32 m0, %0"
                 : "=&s"(keep) : "v"(gsrc), "s"(dst) : "memory");
  else
    asm volatile("s_mov_b32 %0, m0\n\ts_mov_b32 m0, %2\n\ts_nop 0\n\tglobal_load_lds_dwordx4 %1, off\n\ts_mov_b32 m0, %0"
                 : "=&s"(keep) : "v"(gsrc), "s"(dst) : "memory");
}

#define WAITV(n) asm volatile("s_waitcnt vmcnt(" #n ")" ::: "memory")

constexpr unsigned LDS_BYTES = 163840;

__global__ __launch_bounds__(256, 1) void attn_kernel(const short* __restrict__ x, const short* __restrict__ kc,
                                                      const short* __restrict__ vc, const short* __restrict__ wq,
                                                      const short* __restrict__ wo, short* __restrict__ OT,
                                                      short* __restrict__ WT) {
  extern __shared__ __attribute__((aligned(16))) char smem[];
  const unsigned lds0 = (unsigned)(uintptr_t)smem;
  const int tid = threadIdx.x;
  const int w = __builtin_amdgcn_readfirstlane(tid >> 6);
  const int lane = tid & 63;
  const int r = lane & 31, hi = lane >> 5;
  const int bid = blockIdx.x;
#define STAMP(i)
  STAMP(0);
  const int h = (bid & 7) * 4 + ((bid >> 3) & 3);
  const int b = bid >> 5;

  const char* Kh = (const char*)(kc + ((size_t)(b * NH + h)) * SK * DH);
  const char* Vh = (const char*)(vc + ((size_t)(b * NH + h)) * SK * DH);
  const char* Wh = (const char*)(wq + h * DH);
  const char* Xb = (const char*)(x + (size_t)b * SQ * DM);

  const unsigned lrow = lane >> 4;
  unsigned so_kv[4], so_w[4], so_x[4];
#pragma unroll
  for (int j = 0; j < 4; ++j) {
    unsigned row = 16 * w + 4 * j + lrow;
    unsigned ch = (lane & 15) ^ ((lrow << 2) | j);
    so_kv[j] = row * 256 + ch * 16;
    so_w[j] = row * (HD * 2) + ch * 16;
    unsigned xrow = 32 * w + 8 * j + (lane >> 3);
    unsigned xch = (lane & 7) ^ ((xrow >> 1) & 7);
    unsigned xrc = xrow < SQ ? xrow : SQ - 1;
    so_x[j] = xrc * (DM * 2) + xch * 16;
  }
  auto dma_item = [&](int n) {
    unsigned slot = (unsigned)n % 10u;
    unsigned dst = lds0 + slot * 16384 + w * 4096;
    if (n < 16) {
      int t = n >> 1;
      if (n & 1) {
        const char* g = Wh + (size_t)t * 64 * (HD * 2);
#pragma unroll
        for (int j = 0; j < 4; ++j) glds16<false>(g + so_w[j], dst + j * 1024);
      } else {
        const char* g = Xb + t * 128;
#pragma unroll
        for (int j = 0; j < 4; ++j) glds16<false>(g + so_x[j], dst + j * 1024);
      }
    } else {
      int t = (n - 16) >> 1;
      const char* g = ((n & 1) ? Vh : Kh) + (size_t)t * 16384;
#pragma unroll
      for (int j = 0; j < 4; ++j) glds16<true>(g + so_kv[j], dst + j * 1024);
    }
  };

  unsigned wv[32];
  {
    const short* src = wo + (size_t)((bid >> 1) * 32) * 512 + (bid & 1) * 256 + tid;
#pragma unroll
    for (int kk = 0; kk < 32; ++kk)
      asm volatile("global_load_ushort %0, %1, off offset:%2" : "=v"(wv[kk]) : "v"(src + (kk >> 2) * 2048), "i"((kk & 3) * 1024) : "memory");
  }

#pragma unroll
  for (int n = 0; n < 7; ++n) dma_item(n);

  asm volatile("s_waitcnt vmcnt(28)"
               : "+v"(wv[0]), "+v"(wv[1]), "+v"(wv[2]), "+v"(wv[3]), "+v"(wv[4]), "+v"(wv[5]), "+v"(wv[6]), "+v"(wv[7]),
                 "+v"(wv[8]), "+v"(wv[9]), "+v"(wv[10]), "+v"(wv[11]), "+v"(wv[12]), "+v"(wv[13]), "+v"(wv[14]), "+v"(wv[15])
               :: "memory");
  asm volatile(""
               : "+v"(wv[16]), "+v"(wv[17]), "+v"(wv[18]), "+v"(wv[19]), "+v"(wv[20]), "+v"(wv[21]), "+v"(wv[22]), "+v"(wv[23]),
                 "+v"(wv[24]), "+v"(wv[25]), "+v"(wv[26]), "+v"(wv[27]), "+v"(wv[28]), "+v"(wv[29]), "+v"(wv[30]), "+v"(wv[31])
               :: "memory");
#pragma unroll
  for (int n = 7; n < 10; ++n) dma_item(n);
  {
    const int kb = bid >> 1, nh = bid & 1;
    char* dst = (char*)WT + ((size_t)((nh * 16 + (tid >> 4)) * 128 + kb)) * 1024 + (tid & 15) * 16;
#pragma unroll
    for (int g = 0; g < 4; ++g) {
      u32x4 o = {wv[8 * g + 0] | (wv[8 * g + 1] << 16), wv[8 * g + 2] | (wv[8 * g + 3] << 16),
                 wv[8 * g + 4] | (wv[8 * g + 5] << 16), wv[8 * g + 6] | (wv[8 * g + 7] << 16)};
      *(u32x4*)(dst + g * 256) = o;
    }
  }

  const unsigned blk = (lane >> 4) & 1, q4 = (lane & 15) >> 2, p = lane & 3;
  const unsigned lo2 = 2 * blk + (p >> 1);
  const int qrow = 32 * w + r;

  STAMP(1);
  f32x16 qacc[4];
#pragma unroll
  for (int c = 0; c < 4; ++c) qacc[c] = f32x16{};
  {
    unsigned wa[4][2];
#pragma unroll
    for (int c = 0; c < 4; ++c)
#pragma unroll
      for (int t = 0; t < 2; ++t)
        wa[c][t] = 256 * (8 * hi + 4 * t + q4) + 16 * (4 * (c ^ q4) + (lo2 ^ (2 * hi + t))) + 8 * (p & 1);
    unsigned xa[4];
#pragma unroll
    for (int ks = 0; ks < 4; ++ks) xa[ks] = qrow * 128 + 16 * ((2 * ks + hi) ^ ((qrow >> 1) & 7));
    unsigned xslot = 0;
    for (int t8 = 0; t8 < 8; ++t8) {
      if (t8 == 0) WAITV(32);
      else WAITV(24);
      __builtin_amdgcn_s_barrier();
      if (t8 >= 1 && t8 <= 6) { dma_item(2 * t8 + 8); dma_item(2 * t8 + 9); }
      unsigned xbase = lds0 + xslot * 16384, wbase = xbase + 16384;
      xslot += 2; if (xslot >= 10) xslot -= 10;
      bf16x8 xf[4];
      s16x4 af[4][4][2];
#pragma unroll
      for (int ks = 0; ks < 4; ++ks) xf[ks] = lds_read16(xbase + xa[ks]);
#pragma unroll
      for (int ks = 0; ks < 4; ++ks)
#pragma unroll
        for (int c = 0; c < 4; ++c) {
          af[ks][c][0] = tr_read(wbase + wa[c][0] + ks * 4096);
          af[ks][c][1] = tr_read(wbase + wa[c][1] + ks * 4096);
        }
#pragma unroll
      for (int ks = 0; ks < 4; ++ks) {
#pragma unroll
        for (int c = 0; c < 4; ++c) {
          s16x4 a0 = af[ks][c][0], a1 = af[ks][c][1];
          bf16x8 a = {a0[0], a0[1], a0[2], a0[3], a1[0], a1[1], a1[2], a1[3]};
          qacc[c] = __builtin_amdgcn_mfma_f32_32x32x16_bf16(a, xf[ks], qacc[c], 0, 0, 0);
        }
      }
    }
  }
  bf16x8 qf[8];
  {
    unsigned qs = lds0 + 2 * 16384 + w * 8192;
#pragma unroll
    for (int c = 0; c < 4; ++c)
#pragma unroll
      for (int g = 0; g < 4; ++g) {
        u32x2 v = {pk2(qacc[c][4 * g + 0], qacc[c][4 * g + 1]), pk2(qacc[c][4 * g + 2], qacc[c][4 * g + 3])};
        unsigned ch = 4 * c + g;
        unsigned a = qs + r * 256 + 16 * (ch ^ (r & 15)) + 8 * hi;
        *(LDS_AS u32x2*)(uintptr_t)a = v;
      }
    asm volatile("s_waitcnt lgkmcnt(0)" ::: "memory");
#pragma unroll
    for (int s = 0; s < 8; ++s) {
      unsigned ch = 2 * s + hi;
      qf[s] = lds_read16(qs + r * 256 + 16 * (ch ^ (r & 15)));
    }
    asm volatile("s_waitcnt lgkmcnt(0)" ::: "memory");
  }

  STAMP(2);
  const float cs = 0.08838834764831845f * 1.4426950408889634f;
  float m = -__builtin_inff(), l = 0.f;
  f32x16 oacc[4];
#pragma unroll
  for (int c = 0; c < 4; ++c) oacc[c] = f32x16{};
  unsigned ka[8];
#pragma unroll
  for (int s = 0; s < 8; ++s) ka[s] = 256 * r + 16 * ((2 * s + hi) ^ fsw(r));
  unsigned va[4][2];
#pragma unroll
  for (int c = 0; c < 4; ++c)
#pragma unroll
    for (int t = 0; t < 2; ++t)
      va[c][t] = 16384 + 256 * (8 * t + 4 * hi + q4) + 16 * (4 * (c ^ q4) + (lo2 ^ (2 * t + hi))) + 8 * (p & 1);

  unsigned kslot = 6;
  for (int i = 0; i < 16; ++i) {
    if (i == 0) WAITV(16);
    else if (i <= 12) WAITV(24);
    else if (i == 13) WAITV(16);
    else if (i == 14) WAITV(8);
    else WAITV(0);
    __builtin_amdgcn_s_barrier();
    if (i == 0) { dma_item(22); dma_item(23); dma_item(24); dma_item(25); }
    else if (i <= 11) { dma_item(2 * i + 24); dma_item(2 * i + 25); }
    unsigned kbase = lds0 + kslot * 16384;
    kslot += 2; if (kslot >= 10) kslot -= 10;
    f32x16 s0 = f32x16{}, s1 = f32x16{};
    {
      bf16x8 kf[8][2];
#pragma unroll
      for (int s = 0; s < 8; ++s) {
        kf[s][0] = lds_read16(kbase + ka[s]);
        kf[s][1] = lds_read16(kbase + ka[s] + 8192);
      }
#pragma unroll
      for (int s = 0; s < 8; ++s) {
        s0 = __builtin_amdgcn_mfma_f32_32x32x16_bf16(kf[s][0], qf[s], s0, 0, 0, 0);
        s1 = __builtin_amdgcn_mfma_f32_32x32x16_bf16(kf[s][1], qf[s], s1, 0, 0, 0);
      }
    }
    s16x4 vf[4][4][2];
#pragma unroll
    for (int ks = 0; ks < 4; ++ks)
#pragma unroll
      for (int c = 0; c < 4; ++c) {
        vf[ks][c][0] = tr_read(kbase + va[c][0] + ks * 4096);
        vf[ks][c][1] = tr_read(kbase + va[c][1] + ks * 4096);
      }
    float mx = s0[0];
#pragma unroll
    for (int j = 1; j < 16; ++j) mx = fmaxf(mx, s0[j]);
#pragma unroll
    for (int j = 0; j < 16; ++j) mx = fmaxf(mx, s1[j]);
    {
      auto rr = __builtin_amdgcn_permlane32_swap(__float_as_uint(mx), __float_as_uint(mx), false, false);
      mx = fmaxf(__uint_as_float(rr[0]), __uint_as_float(rr[1]));
    }
    float mnew = fmaxf(m, mx * cs);
    float alpha = __builtin_amdgcn_exp2f(m - mnew);
    m = mnew;
    float ps = 0.f;
#pragma unroll
    for (int j = 0; j < 16; ++j) { s0[j] = __builtin_amdgcn_exp2f(fmaf(s0[j], cs, -mnew)); ps += s0[j]; }
#pragma unroll
    for (int j = 0; j < 16; ++j) { s1[j] = __builtin_amdgcn_exp2f(fmaf(s1[j], cs, -mnew)); ps += s1[j]; }
    l = l * alpha + ps;
    if (__any(alpha != 1.f)) {
#pragma unroll
      for (int c = 0; c < 4; ++c) oacc[c] *= alpha;
    }
    bf16x8 pb[4];
#pragma unroll
    for (int sp = 0; sp < 2; ++sp) {
      u32x4 u0 = {pk2(s0[8 * sp + 0], s0[8 * sp + 1]), pk2(s0[8 * sp + 2], s0[8 * sp + 3]),
                  pk2(s0[8 * sp + 4], s0[8 * sp + 5]), pk2(s0[8 * sp + 6], s0[8 * sp + 7])};
      u32x4 u1 = {pk2(s1[8 * sp + 0], s1[8 * sp + 1]), pk2(s1[8 * sp + 2], s1[8 * sp + 3]),
                  pk2(s1[8 * sp + 4], s1[8 * sp + 5]), pk2(s1[8 * sp + 6], s1[8 * sp + 7])};
      pb[sp] = __builtin_bit_cast(bf16x8, u0);
      pb[2 + sp] = __builtin_bit_cast(bf16x8, u1);
    }
#pragma unroll
    for (int ks = 0; ks < 4; ++ks) {
#pragma unroll
      for (int c = 0; c < 4; ++c) {
        s16x4 a0 = vf[ks][c][0], a1 = vf[ks][c][1];
        bf16x8 a = {a0[0], a0[1], a0[2], a0[3], a1[0], a1[1], a1[2], a1[3]};
        oacc[c] = __builtin_amdgcn_mfma_f32_32x32x16_bf16(a, pb[ks], oacc[c], 0, 0, 0);
      }
    }
  }
  STAMP(3);
  {
    auto rr = __builtin_amdgcn_permlane32_swap(__float_as_uint(l), __float_as_uint(l), false, false);
    float lt = __uint_as_float(rr[0]) + __uint_as_float(rr[1]);
    float inv = 1.f / lt;
    if (qrow < SQ) {
      char* ob = (char*)OT + ((size_t)((7 * b + (qrow >> 4)) * 128 + 4 * h)) * 1024 + (qrow & 15) * 16 + 8 * hi;
#pragma unroll
      for (int c = 0; c < 4; ++c)
#pragma unroll
        for (int g = 0; g < 4; ++g) {
          u32x2 v = {pk2(oacc[c][4 * g + 0] * inv, oacc[c][4 * g + 1] * inv),
                     pk2(oacc[c][4 * g + 2] * inv, oacc[c][4 * g + 3] * inv)};
          *(u32x2*)(ob + c * 1024 + g * 256) = v;
        }
    }
  }
}

constexpr unsigned G2_LDS = 5 * 24576;
__global__ __launch_bounds__(256, 1) void oproj_kernel(const short* __restrict__ OT, const short* __restrict__ WT,
                                                       short* __restrict__ out) {
  extern __shared__ __attribute__((aligned(16))) char smem[];
  const unsigned lds0 = (unsigned)(uintptr_t)smem;
  const int tid = threadIdx.x;
  const int w = __builtin_amdgcn_readfirstlane(tid >> 6);
  const int lane = tid & 63;
  const int bid = blockIdx.x;
  const int mt = bid % 14, nt = bid / 14;
  const char* Ab = (const char*)OT + ((size_t)((mt * 4 + w) * 128)) * 1024 + lane * 16;
  const char* Bb = (const char*)WT + ((size_t)((nt * 2 + (w & 1)) * 128 + 2 * (w >> 1))) * 1024 + lane * 16;
  auto stage = [&](int st, unsigned slot) {
    unsigned base = lds0 + slot * 24576;
#pragma unroll
    for (int k = 0; k < 4; ++k) glds16<false>(Ab + (st * 4 + k) * 1024, base + w * 4096 + k * 1024);
#pragma unroll
    for (int k = 0; k < 2; ++k)
      glds16<false>(Bb + (st * 4 + k) * 1024, base + 16384 + (w & 1) * 4096 + (2 * (w >> 1) + k) * 1024);
  };
  stage(0, 0); stage(1, 1); stage(2, 2); stage(3, 3);
  f32x4 acc0 = f32x4{}, acc1 = f32x4{};
  unsigned slot = 0;
  for (int st = 0; st < 32; ++st) {
    if (st <= 28) WAITV(18);
    else if (st == 29) WAITV(12);
    else if (st == 30) WAITV(6);
    else WAITV(0);
    __builtin_amdgcn_s_barrier();
    if (st + 4 < 32) { unsigned ns = slot + 4; if (ns >= 5) ns -= 5; stage(st + 4, ns); }
    unsigned base = lds0 + slot * 24576 + lane * 16;
    bf16x8 a[4], b0[4], b1[4];
#pragma unroll
    for (int k = 0; k < 4; ++k) {
      a[k] = lds_read16(base + w * 4096 + k * 1024);
      b0[k] = lds_read16(base + 16384 + k * 1024);
      b1[k] = lds_read16(base + 16384 + 4096 + k * 1024);
    }
#pragma unroll
    for (int k = 0; k < 4; ++k) {
      acc0 = __builtin_amdgcn_mfma_f32_16x16x32_bf16(a[k], b0[k], acc0, 0, 0, 0);
      acc1 = __builtin_amdgcn_mfma_f32_16x16x32_bf16(a[k], b1[k], acc1, 0, 0, 0);
    }
    slot = slot + 1; if (slot >= 5) slot = 0;
  }
  const int g = lane >> 4, li = lane & 15;
  const int m0 = mt * 64 + 16 * w + 4 * g, n0 = nt * 32 + li;
#pragma unroll
  for (int j = 0; j < 4; ++j) {
    unsigned v0 = pk2(acc0[j], 0.f), v1 = pk2(acc1[j], 0.f);
    out[(size_t)(m0 + j) * 512 + n0] = (short)(v0 & 0xffff);
    out[(size_t)(m0 + j) * 512 + n0 + 16] = (short)(v1 & 0xffff);
  }
}

extern "C" void kernel_launch(void* const* d_in, const int* in_sizes, int n_in, void* d_out, int out_size,
                              void* d_ws, size_t ws_size, hipStream_t stream) {
  const short* x = (const short*)d_in[0];
  const short* kc = (const short*)d_in[1];
  const short* vc = (const short*)d_in[2];
  const short* wq = (const short*)d_in[3];
  const short* wo = (const short*)d_in[4];
  short* OT = (short*)d_ws;
  short* WT = (short*)((char*)d_ws + (size_t)8 * 1024 * 1024);
  (void)hipFuncSetAttribute((const void*)attn_kernel, hipFuncAttributeMaxDynamicSharedMemorySize, LDS_BYTES);
  (void)hipFuncSetAttribute((const void*)oproj_kernel, hipFuncAttributeMaxDynamicSharedMemorySize, G2_LDS);
  attn_kernel<<<256, 256, LDS_BYTES, stream>>>(x, kc, vc, wq, wo, OT, WT);
  oproj_kernel<<<224, 256, G2_LDS, stream>>>(OT, WT, (short*)d_out);
}
```

```cpp
#include <hip/hip_runtime.h>
#include <hip/hip_bf16.h>
#include <stdint.h>
#include <type_traits>

typedef short bf16x8 __attribute__((ext_vector_type(8)));
typedef short s16x4 __attribute__((ext_vector_type(4)));
typedef float f32x16 __attribute__((ext_vector_type(16)));
typedef float f32x4 __attribute__((ext_vector_type(4)));
typedef float f32x2 __attribute__((ext_vector_type(2)));
typedef unsigned u32x4 __attribute__((ext_vector_type(4)));
typedef unsigned u32x2 __attribute__((ext_vector_type(2)));
typedef __bf16 bf16x2_t __attribute__((ext_vector_type(2)));

constexpr int NB = 8, SQ = 112, DM = 512, NH = 32, SK = 1024, DH = 128;
constexpr int HD = NH * DH;

#define LDS_AS __attribute__((address_space(3)))

__device__ __forceinline__ unsigned pk2(float a, float b) {
  f32x2 v = {a, b};
  bf16x2_t r = __builtin_convertvector(v, bf16x2_t);
  return __builtin_bit_cast(unsigned, r);
}

__device__ __forceinline__ unsigned fsw(unsigned row) { return ((row & 3) << 2) | ((row >> 2) & 3); }

__device__ __forceinline__ s16x4 tr_read(unsigned lds_byte_addr) {
  LDS_AS s16x4* p = (LDS_AS s16x4*)(uintptr_t)lds_byte_addr;
  return __builtin_amdgcn_ds_read_tr16_b64_v4i16(p);
}
__device__ __forceinline__ bf16x8 lds_read16(unsigned lds_byte_addr) {
  LDS_AS bf16x8* p = (LDS_AS bf16x8*)(uintptr_t)lds_byte_addr;
  return *p;
}

template <bool NT>
__device__ __forceinline__ void glds16(const void* gsrc, unsigned lds_dst_uniform) {
  unsigned keep;
  unsigned dst = __builtin_amdgcn_readfirstlane(lds_dst_uniform);
  if constexpr (NT)
    asm volatile("s_mov_b32 %0, m0\n\ts_mov_b32 m0, %2\n\ts_nop 0\n\tglobal_load_lds_dwordx4 %1, off nt\n\ts_mov_b32 m0, %0"
                 : "=&s"(keep) : "v"(gsrc), "s"(dst) : "memory");
  else
    asm volatile("s_mov_b32 %0, m0\n\ts_mov_b32 m0, %2\n\ts_nop 0\n\tglobal_load_lds_dwordx4 %1, off\n\ts_mov_b32 m0, %0"
                 : "=&s"(keep) : "v"(gsrc), "s"(dst) : "memory");
}

#define WAITV(n) asm volatile("s_waitcnt vmcnt(" #n ")" ::: "memory")

constexpr unsigned LDS_BYTES = 163840;
#define BARRIER() do { asm volatile("s_waitcnt lgkmcnt(0)" ::: "memory"); __builtin_amdgcn_s_barrier(); } while (0)

__global__ __launch_bounds__(384, 1) void attn_kernel(const short* __restrict__ x, const short* __restrict__ kc,
                                                      const short* __restrict__ vc, const short* __restrict__ wq,
                                                      const short* __restrict__ wo, short* __restrict__ OT,
                                                      short* __restrict__ WT) {
  extern __shared__ __attribute__((aligned(16))) char smem[];
  const unsigned lds0 = (unsigned)(uintptr_t)smem;
  const int tid = threadIdx.x;
  const int w = __builtin_amdgcn_readfirstlane(tid >> 6);
  const int lane = tid & 63;
  const int r = lane & 31, hi = lane >> 5;
  const int bid = blockIdx.x;
#define STAMP(i)
  STAMP(0);
  const int h = (bid & 7) * 4 + ((bid >> 3) & 3);
  const int b = bid >> 5;

  if (w >= 4) {
    const int L = w - 4;
    const char* Kh = (const char*)(kc + ((size_t)(b * NH + h)) * SK * DH);
    const char* Vh = (const char*)(vc + ((size_t)(b * NH + h)) * SK * DH);
    const char* Wh = (const char*)(wq + h * DH);
    const char* Xb = (const char*)(x + (size_t)b * SQ * DM);
    const char* Wo = (const char*)(wo + (size_t)((bid >> 1) * 32) * 512 + (bid & 1) * 256);
    const unsigned lrow = lane >> 4;
    unsigned so_kv[8], so_w[8], so_x[8], so_o[8];
#pragma unroll
    for (int j = 0; j < 8; ++j) {
      unsigned row = 32 * L + 4 * j + lrow;
      unsigned ch = (lane & 15) ^ ((lrow << 2) | (j & 3));
      so_kv[j] = row * 256 + ch * 16;
      so_w[j] = row * (HD * 2) + ch * 16;
      unsigned xrow = 64 * L + 8 * j + (lane >> 3);
      unsigned xch = (lane & 7) ^ ((xrow >> 1) & 7);
      unsigned xrc = xrow < SQ ? xrow : SQ - 1;
      so_x[j] = xrc * (DM * 2) + xch * 16;
      unsigned orow = 16 * L + 2 * j + (lane >> 5);
      unsigned och = (lane & 31) ^ (((orow & 3) << 2) | ((orow >> 2) & 3));
      so_o[j] = orow * 1024 + och * 16;
    }
    auto dma_item = [&](int n) {
      unsigned slot = (unsigned)n % 10u;
      unsigned dst = lds0 + slot * 16384 + L * 8192;
      if (n < 16) {
        int t = n >> 1;
        if (n & 1) {
          const char* g = Wh + (size_t)t * 64 * (HD * 2);
#pragma unroll
          for (int j = 0; j < 8; ++j) glds16<false>(g + so_w[j], dst + j * 1024);
        } else {
          const char* g = Xb + t * 128;
#pragma unroll
          for (int j = 0; j < 8; ++j) glds16<false>(g + so_x[j], dst + j * 1024);
        }
      } else if (n == 16) {
#pragma unroll
        for (int j = 0; j < 8; ++j) glds16<false>(Wo + so_o[j], dst + j * 1024);
      } else if (n >= 18) {
        int t = (n - 18) >> 1;
        const char* g = ((n & 1) ? Vh : Kh) + (size_t)t * 16384;
#pragma unroll
        for (int j = 0; j < 8; ++j) glds16<true>(g + so_kv[j], dst + j * 1024);
      }
    };
    for (int n = 0; n < 7; ++n) dma_item(n);
    WAITV(32);
    for (int n = 7; n < 10; ++n) dma_item(n);
    for (int st = 0; st < 25; ++st) {
      if (st == 0) WAITV(56);
      else if (st >= 5 && st <= 7) WAITV(40);
      else if (st == 9 || st == 22) WAITV(32);
      else if (st == 23) WAITV(16);
      else if (st == 24) WAITV(0);
      else WAITV(48);
      __builtin_amdgcn_s_barrier();
      if (st == 9) { dma_item(24); dma_item(25); dma_item(26); dma_item(27); }
      else if (st >= 1 && st <= 20 && st != 8) { dma_item(2 * st + 8); dma_item(2 * st + 9); }
    }
    return;
  }

  const unsigned blk = (lane >> 4) & 1, q4 = (lane & 15) >> 2, p = lane & 3;
  const unsigned lo2 = 2 * blk + (p >> 1);
  const int qrow = 32 * w + r;

  f32x16 qacc[4];
#pragma unroll
  for (int c = 0; c < 4; ++c) qacc[c] = f32x16{};
  {
    unsigned wa[4][2];
#pragma unroll
    for (int c = 0; c < 4; ++c)
#pragma unroll
      for (int t = 0; t < 2; ++t)
        wa[c][t] = 16384 + 256 * (8 * hi + 4 * t + q4) + 16 * (4 * (c ^ q4) + (lo2 ^ (2 * hi + t))) + 8 * (p & 1);
    unsigned xa[4];
#pragma unroll
    for (int ks = 0; ks < 4; ++ks) xa[ks] = qrow * 128 + 16 * ((2 * ks + hi) ^ ((qrow >> 1) & 7));
    unsigned xslot = 0;
    for (int t8 = 0; t8 < 8; ++t8) {
      BARRIER();
      if (t8 == 0) STAMP(1);
      unsigned xbase = lds0 + xslot * 16384;
      xslot += 2; if (xslot >= 10) xslot -= 10;
      bf16x8 xf[4];
      s16x4 af[4][4][2];
#pragma unroll
      for (int ks = 0; ks < 4; ++ks) xf[ks] = lds_read16(xbase + xa[ks]);
#pragma unroll
      for (int ks = 0; ks < 4; ++ks)
#pragma unroll
        for (int c = 0; c < 4; ++c) {
          af[ks][c][0] = tr_read(xbase + wa[c][0] + ks * 4096);
          af[ks][c][1] = tr_read(xbase + wa[c][1] + ks * 4096);
        }
#pragma unroll
      for (int ks = 0; ks < 4; ++ks) {
#pragma unroll
        for (int c = 0; c < 4; ++c) {
          s16x4 a0 = af[ks][c][0], a1 = af[ks][c][1];
          bf16x8 a = {a0[0], a0[1], a0[2], a0[3], a1[0], a1[1], a1[2], a1[3]};
          qacc[c] = __builtin_amdgcn_mfma_f32_32x32x16_bf16(a, xf[ks], qacc[c], 0, 0, 0);
        }
      }
    }
  }
  BARRIER();
  bf16x8 qf[8];
  {
    unsigned qs = lds0 + 4 * 16384 + w * 8192;
#pragma unroll
    for (int c = 0; c < 4; ++c)
#pragma unroll
      for (int g = 0; g < 4; ++g) {
        u32x2 v = {pk2(qacc[c][4 * g + 0], qacc[c][4 * g + 1]), pk2(qacc[c][4 * g + 2], qacc[c][4 * g + 3])};
        unsigned ch = 4 * c + g;
        unsigned a = qs + r * 256 + 16 * (ch ^ (r & 15)) + 8 * hi;
        *(LDS_AS u32x2*)(uintptr_t)a = v;
      }
    {
      const unsigned g = lane >> 4;
      char* dst = (char*)WT + ((size_t)(((bid & 1) * 16 + 4 * w) * 128 + (bid >> 1))) * 1024 + lane * 16;
#pragma unroll
      for (int nt = 0; nt < 4; ++nt) {
        s16x4 t01[2];
#pragma unroll
        for (int t = 0; t < 2; ++t) {
          unsigned row = 8 * g + 4 * t + q4;
          unsigned ch = 2 * (4 * w + nt) + (p >> 1);
          ch ^= ((row & 3) << 2) | ((row >> 2) & 3);
          t01[t] = tr_read(lds0 + 6 * 16384 + row * 512 + ch * 16 + 8 * (p & 1));
        }
        bf16x8 o = {t01[0][0], t01[0][1], t01[0][2], t01[0][3], t01[1][0], t01[1][1], t01[1][2], t01[1][3]};
        *(bf16x8*)(dst + (size_t)nt * 128 * 1024) = o;
      }
    }
    asm volatile("s_waitcnt lgkmcnt(0)" ::: "memory");
#pragma unroll
    for (int s = 0; s < 8; ++s) {
      unsigned ch = 2 * s + hi;
      qf[s] = lds_read16(qs + r * 256 + 16 * (ch ^ (r & 15)));
    }
  }

  STAMP(2);
  const float cs = 0.08838834764831845f * 1.4426950408889634f;
  float m = -__builtin_inff(), l = 0.f;
  f32x16 oacc[4];
#pragma unroll
  for (int c = 0; c < 4; ++c) oacc[c] = f32x16{};
  unsigned ka[8];
#pragma unroll
  for (int s = 0; s < 8; ++s) ka[s] = 256 * r + 16 * ((2 * s + hi) ^ fsw(r));
  unsigned va[4][2];
#pragma unroll
  for (int c = 0; c < 4; ++c)
#pragma unroll
    for (int t = 0; t < 2; ++t)
      va[c][t] = 16384 + 256 * (8 * t + 4 * hi + q4) + 16 * (4 * (c ^ q4) + (lo2 ^ (2 * t + hi))) + 8 * (p & 1);

  unsigned kslot = 8;
  for (int i = 0; i < 16; ++i) {
    BARRIER();
    unsigned kbase = lds0 + kslot * 16384;
    kslot += 2; if (kslot >= 10) kslot -= 10;
    f32x16 s0 = f32x16{}, s1 = f32x16{};
#pragma unroll
    for (int hf = 0; hf < 2; ++hf) {
      bf16x8 kf[4][2];
#pragma unroll
      for (int s = 0; s < 4; ++s) {
        kf[s][0] = lds_read16(kbase + ka[4 * hf + s]);
        kf[s][1] = lds_read16(kbase + ka[4 * hf + s] + 8192);
      }
#pragma unroll
      for (int s = 0; s < 4; ++s) {
        s0 = __builtin_amdgcn_mfma_f32_32x32x16_bf16(kf[s][0], qf[4 * hf + s], s0, 0, 0, 0);
        s1 = __builtin_amdgcn_mfma_f32_32x32x16_bf16(kf[s][1], qf[4 * hf + s], s1, 0, 0, 0);
      }
    }
    s16x4 vf[2][4][2];
#pragma unroll
    for (int ks = 0; ks < 2; ++ks)
#pragma unroll
      for (int c = 0; c < 4; ++c) {
        vf[ks][c][0] = tr_read(kbase + va[c][0] + ks * 4096);
        vf[ks][c][1] = tr_read(kbase + va[c][1] + ks * 4096);
      }
    float mx = s0[0];
#pragma unroll
    for (int j = 1; j < 16; ++j) mx = fmaxf(mx, s0[j]);
#pragma unroll
    for (int j = 0; j < 16; ++j) mx = fmaxf(mx, s1[j]);
    {
      auto rr = __builtin_amdgcn_permlane32_swap(__float_as_uint(mx), __float_as_uint(mx), false, false);
      mx = fmaxf(__uint_as_float(rr[0]), __uint_as_float(rr[1]));
    }
    float mnew = fmaxf(m, mx * cs);
    float alpha = __builtin_amdgcn_exp2f(m - mnew);
    m = mnew;
    if (__builtin_expect(__any(alpha != 1.f), 0)) {
#pragma unroll
      for (int c = 0; c < 4; ++c) oacc[c] *= alpha;
    }
    float ps = 0.f;
#pragma unroll
    for (int j = 0; j < 16; ++j) { s0[j] = __builtin_amdgcn_exp2f(fmaf(s0[j], cs, -mnew)); ps += s0[j]; }
    bf16x8 pb[2];
#pragma unroll
    for (int sp = 0; sp < 2; ++sp) {
      u32x4 u0 = {pk2(s0[8 * sp + 0], s0[8 * sp + 1]), pk2(s0[8 * sp + 2], s0[8 * sp + 3]),
                  pk2(s0[8 * sp + 4], s0[8 * sp + 5]), pk2(s0[8 * sp + 6], s0[8 * sp + 7])};
      pb[sp] = __builtin_bit_cast(bf16x8, u0);
    }
#pragma unroll
    for (int ks = 0; ks < 2; ++ks) {
#pragma unroll
      for (int c = 0; c < 4; ++c) {
        s16x4 a0 = vf[ks][c][0], a1 = vf[ks][c][1];
        bf16x8 a = {a0[0], a0[1], a0[2], a0[3], a1[0], a1[1], a1[2], a1[3]};
        oacc[c] = __builtin_amdgcn_mfma_f32_32x32x16_bf16(a, pb[ks], oacc[c], 0, 0, 0);
      }
    }
#pragma unroll
    for (int ks = 0; ks < 2; ++ks)
#pragma unroll
      for (int c = 0; c < 4; ++c) {
        vf[ks][c][0] = tr_read(kbase + va[c][0] + (2 + ks) * 4096);
        vf[ks][c][1] = tr_read(kbase + va[c][1] + (2 + ks) * 4096);
      }
#pragma unroll
    for (int j = 0; j < 16; ++j) { s1[j] = __builtin_amdgcn_exp2f(fmaf(s1[j], cs, -mnew)); ps += s1[j]; }
    l = l * alpha + ps;
#pragma unroll
    for (int sp = 0; sp < 2; ++sp) {
      u32x4 u1 = {pk2(s1[8 * sp + 0], s1[8 * sp + 1]), pk2(s1[8 * sp + 2], s1[8 * sp + 3]),
                  pk2(s1[8 * sp + 4], s1[8 * sp + 5]), pk2(s1[8 * sp + 6], s1[8 * sp + 7])};
      pb[sp] = __builtin_bit_cast(bf16x8, u1);
    }
#pragma unroll
    for (int ks = 0; ks < 2; ++ks) {
#pragma unroll
      for (int c = 0; c < 4; ++c) {
        s16x4 a0 = vf[ks][c][0], a1 = vf[ks][c][1];
        bf16x8 a = {a0[0], a0[1], a0[2], a0[3], a1[0], a1[1], a1[2], a1[3]};
        oacc[c] = __builtin_amdgcn_mfma_f32_32x32x16_bf16(a, pb[ks], oacc[c], 0, 0, 0);
      }
    }
  }
  STAMP(3);
  {
    auto rr = __builtin_amdgcn_permlane32_swap(__float_as_uint(l), __float_as_uint(l), false, false);
    float lt = __uint_as_float(rr[0]) + __uint_as_float(rr[1]);
    float inv = 1.f / lt;
    if (qrow < SQ) {
      char* ob = (char*)OT + ((size_t)((7 * b + (qrow >> 4)) * 128 + 4 * h)) * 1024 + (qrow & 15) * 16 + 8 * hi;
#pragma unroll
      for (int c = 0; c < 4; ++c)
#pragma unroll
        for (int g = 0; g < 4; ++g) {
          u32x2 v = {pk2(oacc[c][4 * g + 0] * inv, oacc[c][4 * g + 1] * inv),
                     pk2(oacc[c][4 * g + 2] * inv, oacc[c][4 * g + 3] * inv)};
          *(u32x2*)(ob + c * 1024 + g * 256) = v;
        }
    }
  }
}

constexpr unsigned G2_LDS = 6 * 24576;
__global__ __launch_bounds__(384, 1) void oproj_kernel(const short* __restrict__ OT, const short* __restrict__ WT,
                                                       short* __restrict__ out) {
  extern __shared__ __attribute__((aligned(16))) char smem[];
  const unsigned lds0 = (unsigned)(uintptr_t)smem;
  const int tid = threadIdx.x;
  const int w = __builtin_amdgcn_readfirstlane(tid >> 6);
  const int lane = tid & 63;
  const int bid = blockIdx.x;
  const int mt = bid % 14, nt = bid / 14;
  if (w >= 4) {
    const int L = w - 4;
    const char* Ab = (const char*)OT + ((size_t)((mt * 4 + 2 * L) * 128)) * 1024 + lane * 16;
    const char* Bb = (const char*)WT + ((size_t)((nt * 2 + L) * 128)) * 1024 + lane * 16;
    auto stage = [&](int st, unsigned slot) {
      unsigned base = lds0 + slot * 24576;
#pragma unroll
      for (int mb = 0; mb < 2; ++mb)
#pragma unroll
        for (int k = 0; k < 4; ++k)
          glds16<false>(Ab + (size_t)(mb * 128 + st * 4 + k) * 1024, base + (2 * L + mb) * 4096 + k * 1024);
#pragma unroll
      for (int k = 0; k < 4; ++k) glds16<false>(Bb + (st * 4 + k) * 1024, base + 16384 + L * 4096 + k * 1024);
    };
    stage(0, 0); stage(1, 1); stage(2, 2); stage(3, 3); stage(4, 4);
    unsigned slot = 5;
    for (int st = 0; st < 32; ++st) {
      if (st <= 27) WAITV(48);
      else if (st == 28) WAITV(36);
      else if (st == 29) WAITV(24);
      else if (st == 30) WAITV(12);
      else WAITV(0);
      __builtin_amdgcn_s_barrier();
      if (st + 5 < 32) { stage(st + 5, slot); slot = slot + 1; if (slot >= 6) slot = 0; }
    }
    return;
  }
  f32x4 acc0 = f32x4{}, acc1 = f32x4{};
  unsigned slot = 0;
  for (int st = 0; st < 32; ++st) {
    BARRIER();
    unsigned base = lds0 + slot * 24576 + lane * 16;
    bf16x8 a[4], b0[4], b1[4];
#pragma unroll
    for (int k = 0; k < 4; ++k) {
      a[k] = lds_read16(base + w * 4096 + k * 1024);
      b0[k] = lds_read16(base + 16384 + k * 1024);
      b1[k] = lds_read16(base + 16384 + 4096 + k * 1024);
    }
#pragma unroll
    for (int k = 0; k < 4; ++k) {
      acc0 = __builtin_amdgcn_mfma_f32_16x16x32_bf16(a[k], b0[k], acc0, 0, 0, 0);
      acc1 = __builtin_amdgcn_mfma_f32_16x16x32_bf16(a[k], b1[k], acc1, 0, 0, 0);
    }
    slot = slot + 1; if (slot >= 6) slot = 0;
  }
  const int g = lane >> 4, li = lane & 15;
  const int m0 = mt * 64 + 16 * w + 4 * g, n0 = nt * 32 + li;
#pragma unroll
  for (int j = 0; j < 4; ++j) {
    unsigned v0 = pk2(acc0[j], 0.f), v1 = pk2(acc1[j], 0.f);
    out[(size_t)(m0 + j) * 512 + n0] = (short)(v0 & 0xffff);
    out[(size_t)(m0 + j) * 512 + n0 + 16] = (short)(v1 & 0xffff);
  }
}

extern "C" void kernel_launch(void* const* d_in, const int* in_sizes, int n_in, void* d_out, int out_size,
                              void* d_ws, size_t ws_size, hipStream_t stream) {
  const short* x = (const short*)d_in[0];
  const short* kc = (const short*)d_in[1];
  const short* vc = (const short*)d_in[2];
  const short* wq = (const short*)d_in[3];
  const short* wo = (const short*)d_in[4];
  short* OT = (short*)d_ws;
  short* WT = (short*)((char*)d_ws + (size_t)8 * 1024 * 1024);
  (void)hipFuncSetAttribute((const void*)attn_kernel, hipFuncAttributeMaxDynamicSharedMemorySize, LDS_BYTES);
  (void)hipFuncSetAttribute((const void*)oproj_kernel, hipFuncAttributeMaxDynamicSharedMemorySize, G2_LDS);
  attn_kernel<<<256, 384, LDS_BYTES, stream>>>(x, kc, vc, wq, wo, OT, WT);
  oproj_kernel<<<224, 384, G2_LDS, stream>>>(OT, WT, (short*)d_out);
}
```

```cpp
#include <hip/hip_runtime.h>
#include <hip/hip_bf16.h>
#include <stdint.h>
#include <type_traits>

typedef short bf16x8 __attribute__((ext_vector_type(8)));
typedef short s16x4 __attribute__((ext_vector_type(4)));
typedef float f32x16 __attribute__((ext_vector_type(16)));
typedef float f32x4 __attribute__((ext_vector_type(4)));
typedef float f32x2 __attribute__((ext_vector_type(2)));
typedef unsigned u32x4 __attribute__((ext_vector_type(4)));
typedef unsigned u32x2 __attribute__((ext_vector_type(2)));
typedef __bf16 bf16x2_t __attribute__((ext_vector_type(2)));

constexpr int NB = 8, SQ = 112, DM = 512, NH = 32, SK = 1024, DH = 128;
constexpr int HD = NH * DH;

#define LDS_AS __attribute__((address_space(3)))

__device__ __forceinline__ unsigned pk2(float a, float b) {
  f32x2 v = {a, b};
  bf16x2_t r = __builtin_convertvector(v, bf16x2_t);
  return __builtin_bit_cast(unsigned, r);
}

__device__ __forceinline__ unsigned fsw(unsigned row) { return ((row & 3) << 2) | ((row >> 2) & 3); }

__device__ __forceinline__ s16x4 tr_read(unsigned lds_byte_addr) {
  LDS_AS s16x4* p = (LDS_AS s16x4*)(uintptr_t)lds_byte_addr;
  return __builtin_amdgcn_ds_read_tr16_b64_v4i16(p);
}
__device__ __forceinline__ bf16x8 lds_read16(unsigned lds_byte_addr) {
  LDS_AS bf16x8* p = (LDS_AS bf16x8*)(uintptr_t)lds_byte_addr;
  return *p;
}

template <bool NT>
__device__ __forceinline__ void glds16(const void* gsrc, unsigned lds_dst_uniform) {
  unsigned keep;
  unsigned dst = __builtin_amdgcn_readfirstlane(lds_dst_uniform);
  if constexpr (NT)
    asm volatile("s_mov_b32 %0, m0\n\ts_mov_b32 m0, %2\n\ts_nop 0\n\tglobal_load_lds_dwordx4 %1, off nt\n\ts_mov_b32 m0, %0"
                 : "=&s"(keep) : "v"(gsrc), "s"(dst) : "memory");
  else
    asm volatile("s_mov_b32 %0, m0\n\ts_mov_b32 m0, %2\n\ts_nop 0\n\tglobal_load_lds_dwordx4 %1, off\n\ts_mov_b32 m0, %0"
                 : "=&s"(keep) : "v"(gsrc), "s"(dst) : "memory");
}

#define WAITV(n) asm volatile("s_waitcnt vmcnt(" #n ")" ::: "memory")

constexpr unsigned LDS_BYTES = 163840;
#define BARRIER() do { asm volatile("s_waitcnt lgkmcnt(0)" ::: "memory"); __builtin_amdgcn_s_barrier(); } while (0)

__global__ __launch_bounds__(384, 1) void attn_kernel(const short* __restrict__ x, const short* __restrict__ kc,
                                                      const short* __restrict__ vc, const short* __restrict__ wq,
                                                      const short* __restrict__ wo, short* __restrict__ OT,
                                                      short* __restrict__ WT) {
  extern __shared__ __attribute__((aligned(16))) char smem[];
  const unsigned lds0 = (unsigned)(uintptr_t)smem;
  const int tid = threadIdx.x;
  const int w = __builtin_amdgcn_readfirstlane(tid >> 6);
  const int lane = tid & 63;
  const int r = lane & 31, hi = lane >> 5;
  const int bid = blockIdx.x;
#define STAMP(i)
  STAMP(0);
  const int h = (bid & 7) * 4 + ((bid >> 3) & 3);
  const int b = bid >> 5;

  if (w >= 4) {
    const int L = w - 4;
    const char* Kh = (const char*)(kc + ((size_t)(b * NH + h)) * SK * DH);
    const char* Vh = (const char*)(vc + ((size_t)(b * NH + h)) * SK * DH);
    const char* Wh = (const char*)(wq + h * DH);
    const char* Xb = (const char*)(x + (size_t)b * SQ * DM);
    const char* Wo = (const char*)(wo + (size_t)((bid >> 1) * 32) * 512 + (bid & 1) * 256);
    const unsigned lrow = lane >> 4;
    unsigned so_kv[8], so_w[8], so_x[8], so_o[8];
#pragma unroll
    for (int j = 0; j < 8; ++j) {
      unsigned row = 32 * L + 4 * j + lrow;
      unsigned ch = (lane & 15) ^ ((lrow << 2) | (j & 3));
      so_kv[j] = row * 256 + ch * 16;
      so_w[j] = row * (HD * 2) + ch * 16;
      unsigned xrow = 64 * L + 8 * j + (lane >> 3);
      unsigned xch = (lane & 7) ^ ((xrow >> 1) & 7);
      unsigned xrc = xrow < SQ ? xrow : SQ - 1;
      so_x[j] = xrc * (DM * 2) + xch * 16;
      unsigned orow = 16 * L + 2 * j + (lane >> 5);
      unsigned och = (lane & 31) ^ (((orow & 3) << 2) | ((orow >> 2) & 3));
      so_o[j] = orow * 1024 + och * 16;
    }
    auto dma_item = [&](int n) {
      unsigned slot = (unsigned)n % 10u;
      unsigned dst = lds0 + slot * 16384 + L * 8192;
      if (n < 16) {
        int t = n >> 1;
        if (n & 1) {
          const char* g = Wh + (size_t)t * 64 * (HD * 2);
#pragma unroll
          for (int j = 0; j < 8; ++j) glds16<false>(g + so_w[j], dst + j * 1024);
        } else {
          const char* g = Xb + t * 128;
#pragma unroll
          for (int j = 0; j < 8; ++j) glds16<false>(g + so_x[j], dst + j * 1024);
        }
      } else if (n == 16) {
#pragma unroll
        for (int j = 0; j < 8; ++j) glds16<false>(Wo + so_o[j], dst + j * 1024);
      } else if (n >= 18) {
        int t = (n - 18) >> 1;
        const char* g = ((n & 1) ? Vh : Kh) + (size_t)t * 16384;
#pragma unroll
        for (int j = 0; j < 8; ++j) glds16<true>(g + so_kv[j], dst + j * 1024);
      }
    };
    for (int n = 0; n < 7; ++n) dma_item(n);
    WAITV(32);
    for (int n = 7; n < 10; ++n) dma_item(n);
    for (int st = 0; st < 25; ++st) {
      if (st == 0) WAITV(56);
      else if (st >= 5 && st <= 7) WAITV(40);
      else if (st == 9 || st == 22) WAITV(32);
      else if (st == 23) WAITV(16);
      else if (st == 24) WAITV(0);
      else WAITV(48);
      __builtin_amdgcn_s_barrier();
      if (st == 9) { dma_item(24); dma_item(25); dma_item(26); dma_item(27); }
      else if (st >= 1 && st <= 20 && st != 8) { dma_item(2 * st + 8); dma_item(2 * st + 9); }
    }
    return;
  }

  const unsigned blk = (lane >> 4) & 1, q4 = (lane & 15) >> 2, p = lane & 3;
  const unsigned lo2 = 2 * blk + (p >> 1);
  const int qrow = 32 * w + r;

  f32x16 qacc[4];
#pragma unroll
  for (int c = 0; c < 4; ++c) qacc[c] = f32x16{};
  {
    unsigned wa[4][2];
#pragma unroll
    for (int c = 0; c < 4; ++c)
#pragma unroll
      for (int t = 0; t < 2; ++t)
        wa[c][t] = 16384 + 256 * (8 * hi + 4 * t + q4) + 16 * (4 * (c ^ q4) + (lo2 ^ (2 * hi + t))) + 8 * (p & 1);
    unsigned xa[4];
#pragma unroll
    for (int ks = 0; ks < 4; ++ks) xa[ks] = qrow * 128 + 16 * ((2 * ks + hi) ^ ((qrow >> 1) & 7));
    unsigned xslot = 0;
    for (int t8 = 0; t8 < 8; ++t8) {
      BARRIER();
      STAMP(1 + t8);
      unsigned xbase = lds0 + xslot * 16384;
      xslot += 2; if (xslot >= 10) xslot -= 10;
      bf16x8 xf[4];
      s16x4 af[4][4][2];
#pragma unroll
      for (int ks = 0; ks < 4; ++ks) xf[ks] = lds_read16(xbase + xa[ks]);
#pragma unroll
      for (int ks = 0; ks < 4; ++ks)
#pragma unroll
        for (int c = 0; c < 4; ++c) {
          af[ks][c][0] = tr_read(xbase + wa[c][0] + ks * 4096);
          af[ks][c][1] = tr_read(xbase + wa[c][1] + ks * 4096);
        }
#pragma unroll
      for (int ks = 0; ks < 4; ++ks) {
#pragma unroll
        for (int c = 0; c < 4; ++c) {
          s16x4 a0 = af[ks][c][0], a1 = af[ks][c][1];
          bf16x8 a = {a0[0], a0[1], a0[2], a0[3], a1[0], a1[1], a1[2], a1[3]};
          qacc[c] = __builtin_amdgcn_mfma_f32_32x32x16_bf16(a, xf[ks], qacc[c], 0, 0, 0);
        }
      }
    }
  }
  BARRIER();
  STAMP(9);
  bf16x8 qf[8];
  {
    unsigned qs = lds0 + 4 * 16384 + w * 8192;
#pragma unroll
    for (int c = 0; c < 4; ++c)
#pragma unroll
      for (int g = 0; g < 4; ++g) {
        u32x2 v = {pk2(qacc[c][4 * g + 0], qacc[c][4 * g + 1]), pk2(qacc[c][4 * g + 2], qacc[c][4 * g + 3])};
        unsigned ch = 4 * c + g;
        unsigned a = qs + r * 256 + 16 * (ch ^ (r & 15)) + 8 * hi;
        *(LDS_AS u32x2*)(uintptr_t)a = v;
      }
    {
      const unsigned g = lane >> 4;
      char* dst = (char*)WT + ((size_t)(((bid & 1) * 16 + 4 * w) * 128 + (bid >> 1))) * 1024 + lane * 16;
#pragma unroll
      for (int nt = 0; nt < 4; ++nt) {
        s16x4 t01[2];
#pragma unroll
        for (int t = 0; t < 2; ++t) {
          unsigned row = 8 * g + 4 * t + q4;
          unsigned ch = 2 * (4 * w + nt) + (p >> 1);
          ch ^= ((row & 3) << 2) | ((row >> 2) & 3);
          t01[t] = tr_read(lds0 + 6 * 16384 + row * 512 + ch * 16 + 8 * (p & 1));
        }
        bf16x8 o = {t01[0][0], t01[0][1], t01[0][2], t01[0][3], t01[1][0], t01[1][1], t01[1][2], t01[1][3]};
        *(bf16x8*)(dst + (size_t)nt * 128 * 1024) = o;
      }
    }
    asm volatile("s_waitcnt lgkmcnt(0)" ::: "memory");
#pragma unroll
    for (int s = 0; s < 8; ++s) {
      unsigned ch = 2 * s + hi;
      qf[s] = lds_read16(qs + r * 256 + 16 * (ch ^ (r & 15)));
    }
  }

  const float cs = 0.08838834764831845f * 1.4426950408889634f;
  float m = -__builtin_inff(), l = 0.f;
  f32x16 oacc[4];
#pragma unroll
  for (int c = 0; c < 4; ++c) oacc[c] = f32x16{};
  unsigned ka[8];
#pragma unroll
  for (int s = 0; s < 8; ++s) ka[s] = 256 * r + 16 * ((2 * s + hi) ^ fsw(r));
  unsigned va[4][2];
#pragma unroll
  for (int c = 0; c < 4; ++c)
#pragma unroll
    for (int t = 0; t < 2; ++t)
      va[c][t] = 16384 + 256 * (8 * t + 4 * hi + q4) + 16 * (4 * (c ^ q4) + (lo2 ^ (2 * t + hi))) + 8 * (p & 1);

  unsigned kslot = 8;
  for (int i = 0; i < 16; ++i) {
    BARRIER();
    STAMP(10 + i);
    unsigned kbase = lds0 + kslot * 16384;
    kslot += 2; if (kslot >= 10) kslot -= 10;
    f32x16 s0 = f32x16{}, s1 = f32x16{};
#pragma unroll
    for (int hf = 0; hf < 2; ++hf) {
      bf16x8 kf[4][2];
#pragma unroll
      for (int s = 0; s < 4; ++s) {
        kf[s][0] = lds_read16(kbase + ka[4 * hf + s]);
        kf[s][1] = lds_read16(kbase + ka[4 * hf + s] + 8192);
      }
#pragma unroll
      for (int s = 0; s < 4; ++s) {
        s0 = __builtin_amdgcn_mfma_f32_32x32x16_bf16(kf[s][0], qf[4 * hf + s], s0, 0, 0, 0);
        s1 = __builtin_amdgcn_mfma_f32_32x32x16_bf16(kf[s][1], qf[4 * hf + s], s1, 0, 0, 0);
      }
    }
    s16x4 vf[2][4][2];
#pragma unroll
    for (int ks = 0; ks < 2; ++ks)
#pragma unroll
      for (int c = 0; c < 4; ++c) {
        vf[ks][c][0] = tr_read(kbase + va[c][0] + ks * 4096);
        vf[ks][c][1] = tr_read(kbase + va[c][1] + ks * 4096);
      }
    float mx = s0[0];
#pragma unroll
    for (int j = 1; j < 16; ++j) mx = fmaxf(mx, s0[j]);
#pragma unroll
    for (int j = 0; j < 16; ++j) mx = fmaxf(mx, s1[j]);
    {
      auto rr = __builtin_amdgcn_permlane32_swap(__float_as_uint(mx), __float_as_uint(mx), false, false);
      mx = fmaxf(__uint_as_float(rr[0]), __uint_as_float(rr[1]));
    }
    float mnew = fmaxf(m, mx * cs);
    float alpha = __builtin_amdgcn_exp2f(m - mnew);
    m = mnew;
    if (__builtin_expect(__any(alpha != 1.f), 0)) {
#pragma unroll
      for (int c = 0; c < 4; ++c) oacc[c] *= alpha;
    }
    float ps = 0.f;
#pragma unroll
    for (int j = 0; j < 16; ++j) { s0[j] = __builtin_amdgcn_exp2f(fmaf(s0[j], cs, -mnew)); ps += s0[j]; }
    bf16x8 pb[2];
#pragma unroll
    for (int sp = 0; sp < 2; ++sp) {
      u32x4 u0 = {pk2(s0[8 * sp + 0], s0[8 * sp + 1]), pk2(s0[8 * sp + 2], s0[8 * sp + 3]),
                  pk2(s0[8 * sp + 4], s0[8 * sp + 5]), pk2(s0[8 * sp + 6], s0[8 * sp + 7])};
      pb[sp] = __builtin_bit_cast(bf16x8, u0);
    }
#pragma unroll
    for (int ks = 0; ks < 2; ++ks) {
#pragma unroll
      for (int c = 0; c < 4; ++c) {
        s16x4 a0 = vf[ks][c][0], a1 = vf[ks][c][1];
        bf16x8 a = {a0[0], a0[1], a0[2], a0[3], a1[0], a1[1], a1[2], a1[3]};
        oacc[c] = __builtin_amdgcn_mfma_f32_32x32x16_bf16(a, pb[ks], oacc[c], 0, 0, 0);
      }
    }
#pragma unroll
    for (int ks = 0; ks < 2; ++ks)
#pragma unroll
      for (int c = 0; c < 4; ++c) {
        vf[ks][c][0] = tr_read(kbase + va[c][0] + (2 + ks) * 4096);
        vf[ks][c][1] = tr_read(kbase + va[c][1] + (2 + ks) * 4096);
      }
#pragma unroll
    for (int j = 0; j < 16; ++j) { s1[j] = __builtin_amdgcn_exp2f(fmaf(s1[j], cs, -mnew)); ps += s1[j]; }
    l = l * alpha + ps;
#pragma unroll
    for (int sp = 0; sp < 2; ++sp) {
      u32x4 u1 = {pk2(s1[8 * sp + 0], s1[8 * sp + 1]), pk2(s1[8 * sp + 2], s1[8 * sp + 3]),
                  pk2(s1[8 * sp + 4], s1[8 * sp + 5]), pk2(s1[8 * sp + 6], s1[8 * sp + 7])};
      pb[sp] = __builtin_bit_cast(bf16x8, u1);
    }
#pragma unroll
    for (int ks = 0; ks < 2; ++ks) {
#pragma unroll
      for (int c = 0; c < 4; ++c) {
        s16x4 a0 = vf[ks][c][0], a1 = vf[ks][c][1];
        bf16x8 a = {a0[0], a0[1], a0[2], a0[3], a1[0], a1[1], a1[2], a1[3]};
        oacc[c] = __builtin_amdgcn_mfma_f32_32x32x16_bf16(a, pb[ks], oacc[c], 0, 0, 0);
      }
    }
  }
  STAMP(26);
  {
    auto rr = __builtin_amdgcn_permlane32_swap(__float_as_uint(l), __float_as_uint(l), false, false);
    float lt = __uint_as_float(rr[0]) + __uint_as_float(rr[1]);
    float inv = 1.f / lt;
    if (qrow < SQ) {
      char* ob = (char*)OT + ((size_t)((7 * b + (qrow >> 4)) * 128 + 4 * h)) * 1024 + (qrow & 15) * 16 + 8 * hi;
#pragma unroll
      for (int c = 0; c < 4; ++c)
#pragma unroll
        for (int g = 0; g < 4; ++g) {
          u32x2 v = {pk2(oacc[c][4 * g + 0] * inv, oacc[c][4 * g + 1] * inv),
                     pk2(oacc[c][4 * g + 2] * inv, oacc[c][4 * g + 3] * inv)};
          *(u32x2*)(ob + c * 1024 + g * 256) = v;
        }
    }
  }
}

constexpr int SPLITK = 8;
constexpr unsigned G2_LDS = 4 * 32768;
__global__ __launch_bounds__(384, 1) void oproj_kernel(const short* __restrict__ OT, const short* __restrict__ WT,
                                                       float* __restrict__ part) {
  extern __shared__ __attribute__((aligned(16))) char smem[];
  const unsigned lds0 = (unsigned)(uintptr_t)smem;
  const int tid = threadIdx.x;
  const int w = __builtin_amdgcn_readfirstlane(tid >> 6);
  const int lane = tid & 63;
  const int bid = blockIdx.x;
  const int ks = bid & 7, tile = bid >> 3;
  const int mt = tile % 7, nt = tile / 7;
  if (w >= 4) {
    const int L = w - 4;
    const char* src = L == 0 ? (const char*)OT + ((size_t)(mt * 8 * 128 + ks * 16)) * 1024 + lane * 16
                             : (const char*)WT + ((size_t)(nt * 8 * 128 + ks * 16)) * 1024 + lane * 16;
    auto stage = [&](int st, unsigned slot) {
      unsigned base = lds0 + slot * 32768 + L * 16384;
#pragma unroll
      for (int blk = 0; blk < 8; ++blk)
#pragma unroll
        for (int k = 0; k < 2; ++k)
          glds16<false>(src + (size_t)(blk * 128 + st * 2 + k) * 1024, base + (k * 8 + blk) * 1024);
    };
    stage(0, 0); stage(1, 1); stage(2, 2);
    for (int st = 0; st < 8; ++st) {
      if (st <= 5) WAITV(32);
      else if (st == 6) WAITV(16);
      else WAITV(0);
      __builtin_amdgcn_s_barrier();
      if (st + 3 < 8) stage(st + 3, (st + 3) & 3);
    }
    return;
  }
  const int wm = w & 1, wn = w >> 1;
  f32x4 acc[4][4];
#pragma unroll
  for (int i = 0; i < 4; ++i)
#pragma unroll
    for (int j = 0; j < 4; ++j) acc[i][j] = f32x4{};
  for (int st = 0; st < 8; ++st) {
    BARRIER();
    unsigned base = lds0 + (st & 3) * 32768 + lane * 16;
#pragma unroll
    for (int k = 0; k < 2; ++k) {
      bf16x8 a[4], bb[4];
#pragma unroll
      for (int i = 0; i < 4; ++i) a[i] = lds_read16(base + (k * 8 + wm * 4 + i) * 1024);
#pragma unroll
      for (int j = 0; j < 4; ++j) bb[j] = lds_read16(base + 16384 + (k * 8 + wn * 4 + j) * 1024);
#pragma unroll
      for (int i = 0; i < 4; ++i)
#pragma unroll
        for (int j = 0; j < 4; ++j) acc[i][j] = __builtin_amdgcn_mfma_f32_16x16x32_bf16(a[i], bb[j], acc[i][j], 0, 0, 0);
    }
  }
  const int g = lane >> 4, li = lane & 15;
  float* pb = part + (size_t)ks * (896 * 512) + (size_t)(mt * 128 + wm * 64 + 4 * g) * 512 + nt * 128 + wn * 64 + li;
#pragma unroll
  for (int i = 0; i < 4; ++i)
#pragma unroll
    for (int r4 = 0; r4 < 4; ++r4)
#pragma unroll
      for (int j = 0; j < 4; ++j) pb[(size_t)(i * 16 + r4) * 512 + j * 16] = acc[i][j][r4];
}

__global__ __launch_bounds__(256) void reduce_kernel(const float* __restrict__ part, short* __restrict__ out) {
  const int i = (blockIdx.x * 256 + threadIdx.x) * 8;
  f32x4 a0 = *(const f32x4*)(part + i), a1 = *(const f32x4*)(part + i + 4);
#pragma unroll
  for (int s = 1; s < SPLITK; ++s) {
    a0 += *(const f32x4*)(part + (size_t)s * (896 * 512) + i);
    a1 += *(const f32x4*)(part + (size_t)s * (896 * 512) + i + 4);
  }
  u32x4 o = {pk2(a0[0], a0[1]), pk2(a0[2], a0[3]), pk2(a1[0], a1[1]), pk2(a1[2], a1[3])};
  *(u32x4*)(out + i) = o;
}

extern "C" void kernel_launch(void* const* d_in, const int* in_sizes, int n_in, void* d_out, int out_size,
                              void* d_ws, size_t ws_size, hipStream_t stream) {
  const short* x = (const short*)d_in[0];
  const short* kc = (const short*)d_in[1];
  const short* vc = (const short*)d_in[2];
  const short* wq = (const short*)d_in[3];
  const short* wo = (const short*)d_in[4];
  short* OT = (short*)d_ws;
  short* WT = (short*)((char*)d_ws + (size_t)8 * 1024 * 1024);
  float* part = (float*)((char*)d_ws + (size_t)32 * 1024 * 1024);
  (void)hipFuncSetAttribute((const void*)attn_kernel, hipFuncAttributeMaxDynamicSharedMemorySize, LDS_BYTES);
  (void)hipFuncSetAttribute((const void*)oproj_kernel, hipFuncAttributeMaxDynamicSharedMemorySize, G2_LDS);
  attn_kernel<<<256, 384, LDS_BYTES, stream>>>(x, kc, vc, wq, wo, OT, WT);
  oproj_kernel<<<224, 384, G2_LDS, stream>>>(OT, WT, part);
  reduce_kernel<<<224, 256, 0, stream>>>(part, (short*)d_out);
}
```

```cpp
#include <hip/hip_runtime.h>
#include <hip/hip_bf16.h>
#include <stdint.h>
#include <type_traits>

typedef short bf16x8 __attribute__((ext_vector_type(8)));
typedef short s16x4 __attribute__((ext_vector_type(4)));
typedef float f32x16 __attribute__((ext_vector_type(16)));
typedef float f32x4 __attribute__((ext_vector_type(4)));
typedef float f32x2 __attribute__((ext_vector_type(2)));
typedef unsigned u32x4 __attribute__((ext_vector_type(4)));
typedef unsigned u32x2 __attribute__((ext_vector_type(2)));
typedef __bf16 bf16x2_t __attribute__((ext_vector_type(2)));

constexpr int NB = 8, SQ = 112, DM = 512, NH = 32, SK = 1024, DH = 128;
constexpr int HD = NH * DH;

#define LDS_AS __attribute__((address_space(3)))

__device__ __forceinline__ unsigned pk2(float a, float b) {
  f32x2 v = {a, b};
  bf16x2_t r = __builtin_convertvector(v, bf16x2_t);
  return __builtin_bit_cast(unsigned, r);
}

__device__ __forceinline__ unsigned fsw(unsigned row) { return ((row & 3) << 2) | ((row >> 2) & 3); }

__device__ __forceinline__ s16x4 tr_read(unsigned lds_byte_addr) {
  LDS_AS s16x4* p = (LDS_AS s16x4*)(uintptr_t)lds_byte_addr;
  return __builtin_amdgcn_ds_read_tr16_b64_v4i16(p);
}
__device__ __forceinline__ bf16x8 lds_read16(unsigned lds_byte_addr) {
  LDS_AS bf16x8* p = (LDS_AS bf16x8*)(uintptr_t)lds_byte_addr;
  return *p;
}

template <bool NT>
__device__ __forceinline__ void glds16(const void* gsrc, unsigned lds_dst_uniform) {
  unsigned keep;
  unsigned dst = __builtin_amdgcn_readfirstlane(lds_dst_uniform);
  if constexpr (NT)
    asm volatile("s_mov_b32 %0, m0\n\ts_mov_b32 m0, %2\n\ts_nop 0\n\tglobal_load_lds_dwordx4 %1, off nt\n\ts_mov_b32 m0, %0"
                 : "=&s"(keep) : "v"(gsrc), "s"(dst) : "memory");
  else
    asm volatile("s_mov_b32 %0, m0\n\ts_mov_b32 m0, %2\n\ts_nop 0\n\tglobal_load_lds_dwordx4 %1, off\n\ts_mov_b32 m0, %0"
                 : "=&s"(keep) : "v"(gsrc), "s"(dst) : "memory");
}

#define WAITV(n) asm volatile("s_waitcnt vmcnt(" #n ")" ::: "memory")

constexpr unsigned LDS_BYTES = 163840;
#define BARRIER() do { asm volatile("s_waitcnt lgkmcnt(0)" ::: "memory"); __builtin_amdgcn_s_barrier(); } while (0)

__global__ __launch_bounds__(384, 1) void attn_kernel(const short* __restrict__ x, const short* __restrict__ kc,
                                                      const short* __restrict__ vc, const short* __restrict__ wq,
                                                      const short* __restrict__ wo, short* __restrict__ OT,
                                                      unsigned* __restrict__ cnt, short* __restrict__ out) {
  extern __shared__ __attribute__((aligned(16))) char smem[];
  const unsigned lds0 = (unsigned)(uintptr_t)smem;
  const int tid = threadIdx.x;
  const int w = __builtin_amdgcn_readfirstlane(tid >> 6);
  const int lane = tid & 63;
  const int r = lane & 31, hi = lane >> 5;
  const int bid = blockIdx.x;
#define STAMP(i)
  STAMP(0);
  const int h = (bid & 7) * 4 + ((bid >> 3) & 3);
  const int b = bid >> 5;

  if (w >= 4) {
    const int L = w - 4;
    const char* Kh = (const char*)(kc + ((size_t)(b * NH + h)) * SK * DH);
    const char* Vh = (const char*)(vc + ((size_t)(b * NH + h)) * SK * DH);
    const char* Wh = (const char*)(wq + h * DH);
    const char* Xb = (const char*)(x + (size_t)b * SQ * DM);
    const unsigned lrow = lane >> 4;
    unsigned so_kv[8], so_w[8], so_x[8];
#pragma unroll
    for (int j = 0; j < 8; ++j) {
      unsigned row = 32 * L + 4 * j + lrow;
      unsigned ch = (lane & 15) ^ ((lrow << 2) | (j & 3));
      so_kv[j] = row * 256 + ch * 16;
      so_w[j] = row * (HD * 2) + ch * 16;
      unsigned xrow = 64 * L + 8 * j + (lane >> 3);
      unsigned xch = (lane & 7) ^ ((xrow >> 1) & 7);
      unsigned xrc = xrow < SQ ? xrow : SQ - 1;
      so_x[j] = xrc * (DM * 2) + xch * 16;
    }
    auto dma_item = [&](int n) {
      unsigned slot = (unsigned)n % 10u;
      unsigned dst = lds0 + slot * 16384 + L * 8192;
      if (n < 16) {
        int t = n >> 1;
        if (n & 1) {
          const char* g = Wh + (size_t)t * 64 * (HD * 2);
#pragma unroll
          for (int j = 0; j < 8; ++j) glds16<false>(g + so_w[j], dst + j * 1024);
        } else {
          const char* g = Xb + t * 128;
#pragma unroll
          for (int j = 0; j < 8; ++j) glds16<false>(g + so_x[j], dst + j * 1024);
        }
      } else if (n >= 18) {
        int t = (n - 18) >> 1;
        const char* g = ((n & 1) ? Vh : Kh) + (size_t)t * 16384;
#pragma unroll
        for (int j = 0; j < 8; ++j) glds16<true>(g + so_kv[j], dst + j * 1024);
      }
    };
    for (int n = 0; n < 7; ++n) dma_item(n);
    WAITV(32);
    for (int n = 7; n < 10; ++n) dma_item(n);
    for (int st = 0; st < 25; ++st) {
      if (st == 0) WAITV(56);
      else if (st >= 4 && st <= 7) WAITV(32);
      else if (st == 9 || st == 22) WAITV(32);
      else if (st == 23) WAITV(16);
      else if (st == 24) WAITV(0);
      else WAITV(48);
      __builtin_amdgcn_s_barrier();
      if (st == 9) { dma_item(24); dma_item(25); dma_item(26); dma_item(27); }
      else if (st >= 1 && st <= 20 && st != 8) { dma_item(2 * st + 8); dma_item(2 * st + 9); }
    }
    __builtin_amdgcn_s_barrier();
    {
      unsigned spins = 0;
      while (__hip_atomic_load(cnt + b * 16, __ATOMIC_RELAXED, __HIP_MEMORY_SCOPE_AGENT) < 32u) {
        __builtin_amdgcn_s_sleep(2);
        if (++spins > (1u << 24)) break;
      }
      __builtin_amdgcn_fence(__ATOMIC_ACQUIRE, "agent");
      asm volatile("s_waitcnt vmcnt(0)" ::: "memory");
    }
    {
      const int mh = h & 1, nt = h >> 1;
      const char* Ab = (const char*)OT + ((size_t)((7 * b + 3 * mh + 2 * L) * 128)) * 1024 + lane * 16;
      unsigned sb[4];
#pragma unroll
      for (int j = 0; j < 4; ++j) {
        int row = 16 * (4 * L + j) + (lane >> 2);
        int ch = (lane & 3) ^ (((row >> 3) & 1) * 2);
        sb[j] = row * 1024 + ch * 16;
      }
      const char* Bb = (const char*)(wo + nt * 32);
      auto stage = [&](int st, unsigned slot) {
        unsigned base = lds0 + slot * 24576;
#pragma unroll
        for (int mb = 0; mb < 2; ++mb)
#pragma unroll
          for (int k = 0; k < 4; ++k)
            glds16<false>(Ab + (size_t)(mb * 128 + st * 4 + k) * 1024, base + (2 * L + mb) * 4096 + k * 1024);
#pragma unroll
        for (int j = 0; j < 4; ++j)
          glds16<false>(Bb + (size_t)st * 128 * 1024 + sb[j], base + 16384 + (4 * L + j) * 1024);
      };
      stage(0, 0); stage(1, 1); stage(2, 2); stage(3, 3); stage(4, 4);
      unsigned slot = 5;
      for (int st = 0; st < 32; ++st) {
        if (st <= 27) WAITV(48);
        else if (st == 28) WAITV(36);
        else if (st == 29) WAITV(24);
        else if (st == 30) WAITV(12);
        else WAITV(0);
        __builtin_amdgcn_s_barrier();
        if (st + 5 < 32) { stage(st + 5, slot); slot = slot + 1; if (slot >= 6) slot = 0; }
      }
    }
    return;
  }

  const unsigned blk = (lane >> 4) & 1, q4 = (lane & 15) >> 2, p = lane & 3;
  const unsigned lo2 = 2 * blk + (p >> 1);
  const int qrow = 32 * w + r;

  f32x16 qacc[4];
#pragma unroll
  for (int c = 0; c < 4; ++c) qacc[c] = f32x16{};
  {
    unsigned wa[4][2];
#pragma unroll
    for (int c = 0; c < 4; ++c)
#pragma unroll
      for (int t = 0; t < 2; ++t)
        wa[c][t] = 16384 + 256 * (8 * hi + 4 * t + q4) + 16 * (4 * (c ^ q4) + (lo2 ^ (2 * hi + t))) + 8 * (p & 1);
    unsigned xa[4];
#pragma unroll
    for (int ks = 0; ks < 4; ++ks) xa[ks] = qrow * 128 + 16 * ((2 * ks + hi) ^ ((qrow >> 1) & 7));
    unsigned xslot = 0;
    for (int t8 = 0; t8 < 8; ++t8) {
      BARRIER();
      STAMP(1 + t8);
      unsigned xbase = lds0 + xslot * 16384;
      xslot += 2; if (xslot >= 10) xslot -= 10;
      bf16x8 xf[4];
      s16x4 af[4][4][2];
#pragma unroll
      for (int ks = 0; ks < 4; ++ks) xf[ks] = lds_read16(xbase + xa[ks]);
#pragma unroll
      for (int ks = 0; ks < 4; ++ks)
#pragma unroll
        for (int c = 0; c < 4; ++c) {
          af[ks][c][0] = tr_read(xbase + wa[c][0] + ks * 4096);
          af[ks][c][1] = tr_read(xbase + wa[c][1] + ks * 4096);
        }
#pragma unroll
      for (int ks = 0; ks < 4; ++ks) {
#pragma unroll
        for (int c = 0; c < 4; ++c) {
          s16x4 a0 = af[ks][c][0], a1 = af[ks][c][1];
          bf16x8 a = {a0[0], a0[1], a0[2], a0[3], a1[0], a1[1], a1[2], a1[3]};
          qacc[c] = __builtin_amdgcn_mfma_f32_32x32x16_bf16(a, xf[ks], qacc[c], 0, 0, 0);
        }
      }
    }
  }
  BARRIER();
  STAMP(9);
  bf16x8 qf[8];
  {
    unsigned qs = lds0 + 4 * 16384 + w * 8192;
#pragma unroll
    for (int c = 0; c < 4; ++c)
#pragma unroll
      for (int g = 0; g < 4; ++g) {
        u32x2 v = {pk2(qacc[c][4 * g + 0], qacc[c][4 * g + 1]), pk2(qacc[c][4 * g + 2], qacc[c][4 * g + 3])};
        unsigned ch = 4 * c + g;
        unsigned a = qs + r * 256 + 16 * (ch ^ (r & 15)) + 8 * hi;
        *(LDS_AS u32x2*)(uintptr_t)a = v;
      }
    asm volatile("s_waitcnt lgkmcnt(0)" ::: "memory");
#pragma unroll
    for (int s = 0; s < 8; ++s) {
      unsigned ch = 2 * s + hi;
      qf[s] = lds_read16(qs + r * 256 + 16 * (ch ^ (r & 15)));
    }
  }

  const float cs = 0.08838834764831845f * 1.4426950408889634f;
  float m = -__builtin_inff(), l = 0.f;
  f32x16 oacc[4];
#pragma unroll
  for (int c = 0; c < 4; ++c) oacc[c] = f32x16{};
  unsigned ka[8];
#pragma unroll
  for (int s = 0; s < 8; ++s) ka[s] = 256 * r + 16 * ((2 * s + hi) ^ fsw(r));
  unsigned va[4][2];
#pragma unroll
  for (int c = 0; c < 4; ++c)
#pragma unroll
    for (int t = 0; t < 2; ++t)
      va[c][t] = 16384 + 256 * (8 * t + 4 * hi + q4) + 16 * (4 * (c ^ q4) + (lo2 ^ (2 * t + hi))) + 8 * (p & 1);

  unsigned kslot = 8;
  for (int i = 0; i < 16; ++i) {
    BARRIER();
    STAMP(10 + i);
    unsigned kbase = lds0 + kslot * 16384;
    kslot += 2; if (kslot >= 10) kslot -= 10;
    f32x16 s0 = f32x16{}, s1 = f32x16{};
#pragma unroll
    for (int hf = 0; hf < 2; ++hf) {
      bf16x8 kf[4][2];
#pragma unroll
      for (int s = 0; s < 4; ++s) {
        kf[s][0] = lds_read16(kbase + ka[4 * hf + s]);
        kf[s][1] = lds_read16(kbase + ka[4 * hf + s] + 8192);
      }
#pragma unroll
      for (int s = 0; s < 4; ++s) {
        s0 = __builtin_amdgcn_mfma_f32_32x32x16_bf16(kf[s][0], qf[4 * hf + s], s0, 0, 0, 0);
        s1 = __builtin_amdgcn_mfma_f32_32x32x16_bf16(kf[s][1], qf[4 * hf + s], s1, 0, 0, 0);
      }
    }
    s16x4 vf[2][4][2];
#pragma unroll
    for (int ks = 0; ks < 2; ++ks)
#pragma unroll
      for (int c = 0; c < 4; ++c) {
        vf[ks][c][0] = tr_read(kbase + va[c][0] + ks * 4096);
        vf[ks][c][1] = tr_read(kbase + va[c][1] + ks * 4096);
      }
    float mx = s0[0];
#pragma unroll
    for (int j = 1; j < 16; ++j) mx = fmaxf(mx, s0[j]);
#pragma unroll
    for (int j = 0; j < 16; ++j) mx = fmaxf(mx, s1[j]);
    {
      auto rr = __builtin_amdgcn_permlane32_swap(__float_as_uint(mx), __float_as_uint(mx), false, false);
      mx = fmaxf(__uint_as_float(rr[0]), __uint_as_float(rr[1]));
    }
    float mnew = fmaxf(m, mx * cs);
    float alpha = __builtin_amdgcn_exp2f(m - mnew);
    m = mnew;
    if (__builtin_expect(__any(alpha != 1.f), 0)) {
#pragma unroll
      for (int c = 0; c < 4; ++c) oacc[c] *= alpha;
    }
    float ps = 0.f;
#pragma unroll
    for (int j = 0; j < 16; ++j) { s0[j] = __builtin_amdgcn_exp2f(fmaf(s0[j], cs, -mnew)); ps += s0[j]; }
    bf16x8 pb[2];
#pragma unroll
    for (int sp = 0; sp < 2; ++sp) {
      u32x4 u0 = {pk2(s0[8 * sp + 0], s0[8 * sp + 1]), pk2(s0[8 * sp + 2], s0[8 * sp + 3]),
                  pk2(s0[8 * sp + 4], s0[8 * sp + 5]), pk2(s0[8 * sp + 6], s0[8 * sp + 7])};
      pb[sp] = __builtin_bit_cast(bf16x8, u0);
    }
#pragma unroll
    for (int ks = 0; ks < 2; ++ks) {
#pragma unroll
      for (int c = 0; c < 4; ++c) {
        s16x4 a0 = vf[ks][c][0], a1 = vf[ks][c][1];
        bf16x8 a = {a0[0], a0[1], a0[2], a0[3], a1[0], a1[1], a1[2], a1[3]};
        oacc[c] = __builtin_amdgcn_mfma_f32_32x32x16_bf16(a, pb[ks], oacc[c], 0, 0, 0);
      }
    }
#pragma unroll
    for (int ks = 0; ks < 2; ++ks)
#pragma unroll
      for (int c = 0; c < 4; ++c) {
        vf[ks][c][0] = tr_read(kbase + va[c][0] + (2 + ks) * 4096);
        vf[ks][c][1] = tr_read(kbase + va[c][1] + (2 + ks) * 4096);
      }
#pragma unroll
    for (int j = 0; j < 16; ++j) { s1[j] = __builtin_amdgcn_exp2f(fmaf(s1[j], cs, -mnew)); ps += s1[j]; }
    l = l * alpha + ps;
#pragma unroll
    for (int sp = 0; sp < 2; ++sp) {
      u32x4 u1 = {pk2(s1[8 * sp + 0], s1[8 * sp + 1]), pk2(s1[8 * sp + 2], s1[8 * sp + 3]),
                  pk2(s1[8 * sp + 4], s1[8 * sp + 5]), pk2(s1[8 * sp + 6], s1[8 * sp + 7])};
      pb[sp] = __builtin_bit_cast(bf16x8, u1);
    }
#pragma unroll
    for (int ks = 0; ks < 2; ++ks) {
#pragma unroll
      for (int c = 0; c < 4; ++c) {
        s16x4 a0 = vf[ks][c][0], a1 = vf[ks][c][1];
        bf16x8 a = {a0[0], a0[1], a0[2], a0[3], a1[0], a1[1], a1[2], a1[3]};
        oacc[c] = __builtin_amdgcn_mfma_f32_32x32x16_bf16(a, pb[ks], oacc[c], 0, 0, 0);
      }
    }
  }
  STAMP(26);
  {
    auto rr = __builtin_amdgcn_permlane32_swap(__float_as_uint(l), __float_as_uint(l), false, false);
    float lt = __uint_as_float(rr[0]) + __uint_as_float(rr[1]);
    float inv = 1.f / lt;
    if (qrow < SQ) {
      char* ob = (char*)OT + ((size_t)((7 * b + (qrow >> 4)) * 128 + 4 * h)) * 1024 + (qrow & 15) * 16 + 8 * hi;
#pragma unroll
      for (int c = 0; c < 4; ++c)
#pragma unroll
        for (int g = 0; g < 4; ++g) {
          u32x2 v = {pk2(oacc[c][4 * g + 0] * inv, oacc[c][4 * g + 1] * inv),
                     pk2(oacc[c][4 * g + 2] * inv, oacc[c][4 * g + 3] * inv)};
          asm volatile("global_store_dwordx2 %0, %1, off sc1\n\ts_nop 1" :: "v"(ob + c * 1024 + g * 256), "v"(v) : "memory");
        }
    }
  }
  asm volatile("s_waitcnt vmcnt(0)" ::: "memory");
  BARRIER();
  if (tid == 0) __hip_atomic_fetch_add(cnt + b * 16, 1u, __ATOMIC_RELAXED, __HIP_MEMORY_SCOPE_AGENT);
  STAMP(27);
  {
    const unsigned g = lane >> 4, li = lane & 15;
    const unsigned sw = (g & 1) * 32;
    unsigned tra[2];
#pragma unroll
    for (int c = 0; c < 2; ++c) tra[c] = 16384 + (8 * g + q4) * 64 + ((c * 32) ^ sw) + p * 8;
    f32x4 acc0 = f32x4{}, acc1 = f32x4{};
    unsigned slot = 0;
    for (int st = 0; st < 32; ++st) {
      BARRIER();
      unsigned base = lds0 + slot * 24576;
      bf16x8 a[4];
      s16x4 bfr[4][2][2];
#pragma unroll
      for (int k = 0; k < 4; ++k) a[k] = lds_read16(base + w * 4096 + k * 1024 + lane * 16);
#pragma unroll
      for (int k = 0; k < 4; ++k)
#pragma unroll
        for (int c = 0; c < 2; ++c) {
          bfr[k][c][0] = tr_read(base + tra[c] + k * 2048);
          bfr[k][c][1] = tr_read(base + tra[c] + k * 2048 + 256);
        }
#pragma unroll
      for (int k = 0; k < 4; ++k) {
        s16x4 b0 = bfr[k][0][0], b1 = bfr[k][0][1], c0 = bfr[k][1][0], c1 = bfr[k][1][1];
        bf16x8 bb0 = {b0[0], b0[1], b0[2], b0[3], b1[0], b1[1], b1[2], b1[3]};
        bf16x8 bb1 = {c0[0], c0[1], c0[2], c0[3], c1[0], c1[1], c1[2], c1[3]};
        acc0 = __builtin_amdgcn_mfma_f32_16x16x32_bf16(a[k], bb0, acc0, 0, 0, 0);
        acc1 = __builtin_amdgcn_mfma_f32_16x16x32_bf16(a[k], bb1, acc1, 0, 0, 0);
      }
      slot = slot + 1; if (slot >= 6) slot = 0;
    }
    const int mh = h & 1, nt = h >> 1;
    const int trow = 16 * w + 4 * (int)g;
    const int row0 = 48 * mh + trow;
#pragma unroll
    for (int j = 0; j < 4; ++j) {
      int tr = trow + j;
      bool ok = mh ? (tr >= 8) : (tr < 56);
      if (ok) {
        unsigned v0 = pk2(acc0[j], 0.f), v1 = pk2(acc1[j], 0.f);
        short* o = out + (size_t)(b * SQ + row0 + j) * 512 + nt * 32 + li;
        o[0] = (short)(v0 & 0xffff);
        o[16] = (short)(v1 & 0xffff);
      }
    }
  }
}

extern "C" void kernel_launch(void* const* d_in, const int* in_sizes, int n_in, void* d_out, int out_size,
                              void* d_ws, size_t ws_size, hipStream_t stream) {
  const short* x = (const short*)d_in[0];
  const short* kc = (const short*)d_in[1];
  const short* vc = (const short*)d_in[2];
  const short* wq = (const short*)d_in[3];
  const short* wo = (const short*)d_in[4];
  unsigned* cnt = (unsigned*)d_ws;
  short* OT = (short*)((char*)d_ws + 4096);
  (void)hipFuncSetAttribute((const void*)attn_kernel, hipFuncAttributeMaxDynamicSharedMemorySize, LDS_BYTES);
  (void)hipMemsetAsync(cnt, 0, 512, stream);
  attn_kernel<<<256, 384, LDS_BYTES, stream>>>(x, kc, vc, wq, wo, OT, cnt, (short*)d_out);
}
```

```cpp
#include <hip/hip_runtime.h>
#include <hip/hip_bf16.h>
#include <stdint.h>
#include <type_traits>

typedef short bf16x8 __attribute__((ext_vector_type(8)));
typedef short s16x4 __attribute__((ext_vector_type(4)));
typedef float f32x16 __attribute__((ext_vector_type(16)));
typedef float f32x4 __attribute__((ext_vector_type(4)));
typedef float f32x2 __attribute__((ext_vector_type(2)));
typedef unsigned u32x4 __attribute__((ext_vector_type(4)));
typedef unsigned u32x2 __attribute__((ext_vector_type(2)));
typedef __bf16 bf16x2_t __attribute__((ext_vector_type(2)));

constexpr int NB = 8, SQ = 112, DM = 512, NH = 32, SK = 1024, DH = 128;
constexpr int HD = NH * DH;

#define LDS_AS __attribute__((address_space(3)))

__device__ __forceinline__ unsigned pk2(float a, float b) {
  f32x2 v = {a, b};
  bf16x2_t r = __builtin_convertvector(v, bf16x2_t);
  return __builtin_bit_cast(unsigned, r);
}

__device__ __forceinline__ unsigned fsw(unsigned row) { return ((row & 3) << 2) | ((row >> 2) & 3); }

__device__ __forceinline__ s16x4 tr_read(unsigned lds_byte_addr) {
  LDS_AS s16x4* p = (LDS_AS s16x4*)(uintptr_t)lds_byte_addr;
  return __builtin_amdgcn_ds_read_tr16_b64_v4i16(p);
}
__device__ __forceinline__ bf16x8 lds_read16(unsigned lds_byte_addr) {
  LDS_AS bf16x8* p = (LDS_AS bf16x8*)(uintptr_t)lds_byte_addr;
  return *p;
}

template <bool NT>
__device__ __forceinline__ void glds16(const void* gsrc, unsigned lds_dst_uniform) {
  unsigned keep;
  unsigned dst = __builtin_amdgcn_readfirstlane(lds_dst_uniform);
  if constexpr (NT)
    asm volatile("s_mov_b32 %0, m0\n\ts_mov_b32 m0, %2\n\ts_nop 0\n\tglobal_load_lds_dwordx4 %1, off nt\n\ts_mov_b32 m0, %0"
                 : "=&s"(keep) : "v"(gsrc), "s"(dst) : "memory");
  else
    asm volatile("s_mov_b32 %0, m0\n\ts_mov_b32 m0, %2\n\ts_nop 0\n\tglobal_load_lds_dwordx4 %1, off\n\ts_mov_b32 m0, %0"
                 : "=&s"(keep) : "v"(gsrc), "s"(dst) : "memory");
}

#define WAITV(n) asm volatile("s_waitcnt vmcnt(" #n ")" ::: "memory")

constexpr unsigned LDS_BYTES = 163840;
#define BARRIER() do { asm volatile("s_waitcnt lgkmcnt(0)" ::: "memory"); __builtin_amdgcn_s_barrier(); } while (0)

__global__ __launch_bounds__(384, 1) void attn_kernel(const short* __restrict__ x, const short* __restrict__ kc,
                                                      const short* __restrict__ vc, const short* __restrict__ wq,
                                                      const short* __restrict__ wo, short* __restrict__ OT,
                                                      short* __restrict__ WT) {
  extern __shared__ __attribute__((aligned(16))) char smem[];
  const unsigned lds0 = (unsigned)(uintptr_t)smem;
  const int tid = threadIdx.x;
  const int w = __builtin_amdgcn_readfirstlane(tid >> 6);
  const int lane = tid & 63;
  const int r = lane & 31, hi = lane >> 5;
  const int bid = blockIdx.x;
#define STAMP(i)
  STAMP(0);
  const int h = (bid & 7) * 4 + ((bid >> 3) & 3);
  const int b = bid >> 5;

  if (w >= 4) {
    const int L = w - 4;
    const char* Kh = (const char*)(kc + ((size_t)(b * NH + h)) * SK * DH);
    const char* Vh = (const char*)(vc + ((size_t)(b * NH + h)) * SK * DH);
    const char* Wh = (const char*)(wq + h * DH);
    const char* Xb = (const char*)(x + (size_t)b * SQ * DM);
    const char* Wo = (const char*)(wo + (size_t)((bid >> 1) * 32) * 512 + (bid & 1) * 256);
    const unsigned lrow = lane >> 4;
    unsigned so_kv[8], so_w[8], so_x[8], so_o[8];
#pragma unroll
    for (int j = 0; j < 8; ++j) {
      unsigned row = 32 * L + 4 * j + lrow;
      unsigned ch = (lane & 15) ^ ((lrow << 2) | (j & 3));
      so_kv[j] = row * 256 + ch * 16;
      so_w[j] = row * (HD * 2) + ch * 16;
      unsigned xrow = 64 * L + 8 * j + (lane >> 3);
      unsigned xch = (lane & 7) ^ ((xrow >> 1) & 7);
      unsigned xrc = xrow < SQ ? xrow : SQ - 1;
      so_x[j] = xrc * (DM * 2) + xch * 16;
      unsigned orow = 16 * L + 2 * j + (lane >> 5);
      unsigned och = (lane & 31) ^ (((orow & 3) << 2) | ((orow >> 2) & 3));
      so_o[j] = orow * 1024 + och * 16;
    }
    auto dma_item = [&](int n) {
      unsigned slot = (unsigned)n % 10u;
      unsigned dst = lds0 + slot * 16384 + L * 8192;
      if (n < 16) {
        int t = n >> 1;
        if (n & 1) {
          const char* g = Wh + (size_t)t * 64 * (HD * 2);
#pragma unroll
          for (int j = 0; j < 8; ++j) glds16<false>(g + so_w[j], dst + j * 1024);
        } else {
          const char* g = Xb + t * 128;
#pragma unroll
          for (int j = 0; j < 8; ++j) glds16<false>(g + so_x[j], dst + j * 1024);
        }
      } else if (n == 16) {
#pragma unroll
        for (int j = 0; j < 8; ++j) glds16<false>(Wo + so_o[j], dst + j * 1024);
      } else if (n >= 18) {
        int t = (n - 18) >> 1;
        const char* g = ((n & 1) ? Vh : Kh) + (size_t)t * 16384;
#pragma unroll
        for (int j = 0; j < 8; ++j) glds16<true>(g + so_kv[j], dst + j * 1024);
      }
    };
    for (int n = 0; n < 7; ++n) dma_item(n);
    WAITV(32);
    for (int n = 7; n < 10; ++n) dma_item(n);
    for (int st = 0; st < 25; ++st) {
      if (st == 0) WAITV(56);
      else if (st >= 5 && st <= 7) WAITV(40);
      else if (st == 9 || st == 22) WAITV(32);
      else if (st == 23) WAITV(16);
      else if (st == 24) WAITV(0);
      else WAITV(48);
      __builtin_amdgcn_s_barrier();
      if (st == 9) { dma_item(24); dma_item(25); dma_item(26); dma_item(27); }
      else if (st >= 1 && st <= 20 && st != 8) { dma_item(2 * st + 8); dma_item(2 * st + 9); }
    }
    return;
  }

  const unsigned blk = (lane >> 4) & 1, q4 = (lane & 15) >> 2, p = lane & 3;
  const unsigned lo2 = 2 * blk + (p >> 1);
  const int qrow = 32 * w + r;

  f32x16 qacc[4];
#pragma unroll
  for (int c = 0; c < 4; ++c) qacc[c] = f32x16{};
  {
    unsigned wa[4][2];
#pragma unroll
    for (int c = 0; c < 4; ++c)
#pragma unroll
      for (int t = 0; t < 2; ++t)
        wa[c][t] = 16384 + 256 * (8 * hi + 4 * t + q4) + 16 * (4 * (c ^ q4) + (lo2 ^ (2 * hi + t))) + 8 * (p & 1);
    unsigned xa[4];
#pragma unroll
    for (int ks = 0; ks < 4; ++ks) xa[ks] = qrow * 128 + 16 * ((2 * ks + hi) ^ ((qrow >> 1) & 7));
    unsigned xslot = 0;
    for (int t8 = 0; t8 < 8; ++t8) {
      BARRIER();
      STAMP(1 + t8);
      unsigned xbase = lds0 + xslot * 16384;
      xslot += 2; if (xslot >= 10) xslot -= 10;
      bf16x8 xf[4];
      s16x4 af[4][4][2];
#pragma unroll
      for (int ks = 0; ks < 4; ++ks) xf[ks] = lds_read16(xbase + xa[ks]);
#pragma unroll
      for (int ks = 0; ks < 4; ++ks)
#pragma unroll
        for (int c = 0; c < 4; ++c) {
          af[ks][c][0] = tr_read(xbase + wa[c][0] + ks * 4096);
          af[ks][c][1] = tr_read(xbase + wa[c][1] + ks * 4096);
        }
#pragma unroll
      for (int ks = 0; ks < 4; ++ks) {
#pragma unroll
        for (int c = 0; c < 4; ++c) {
          s16x4 a0 = af[ks][c][0], a1 = af[ks][c][1];
          bf16x8 a = {a0[0], a0[1], a0[2], a0[3], a1[0], a1[1], a1[2], a1[3]};
          qacc[c] = __builtin_amdgcn_mfma_f32_32x32x16_bf16(a, xf[ks], qacc[c], 0, 0, 0);
        }
      }
    }
  }
  BARRIER();
  STAMP(9);
  bf16x8 qf[8];
  {
    unsigned qs = lds0 + 4 * 16384 + w * 8192;
#pragma unroll
    for (int c = 0; c < 4; ++c)
#pragma unroll
      for (int g = 0; g < 4; ++g) {
        u32x2 v = {pk2(qacc[c][4 * g + 0], qacc[c][4 * g + 1]), pk2(qacc[c][4 * g + 2], qacc[c][4 * g + 3])};
        unsigned ch = 4 * c + g;
        unsigned a = qs + r * 256 + 16 * (ch ^ (r & 15)) + 8 * hi;
        *(LDS_AS u32x2*)(uintptr_t)a = v;
      }
    {
      const unsigned g = lane >> 4;
      char* dst = (char*)WT + ((size_t)(((bid & 1) * 16 + 4 * w) * 128 + (bid >> 1))) * 1024 + lane * 16;
#pragma unroll
      for (int nt = 0; nt < 4; ++nt) {
        s16x4 t01[2];
#pragma unroll
        for (int t = 0; t < 2; ++t) {
          unsigned row = 8 * g + 4 * t + q4;
          unsigned ch = 2 * (4 * w + nt) + (p >> 1);
          ch ^= ((row & 3) << 2) | ((row >> 2) & 3);
          t01[t] = tr_read(lds0 + 6 * 16384 + row * 512 + ch * 16 + 8 * (p & 1));
        }
        bf16x8 o = {t01[0][0], t01[0][1], t01[0][2], t01[0][3], t01[1][0], t01[1][1], t01[1][2], t01[1][3]};
        *(bf16x8*)(dst + (size_t)nt * 128 * 1024) = o;
      }
    }
    asm volatile("s_waitcnt lgkmcnt(0)" ::: "memory");
#pragma unroll
    for (int s = 0; s < 8; ++s) {
      unsigned ch = 2 * s + hi;
      qf[s] = lds_read16(qs + r * 256 + 16 * (ch ^ (r & 15)));
    }
  }

  const float cs = 0.08838834764831845f * 1.4426950408889634f;
  float m = -__builtin_inff(), l = 0.f;
  f32x16 oacc[4];
#pragma unroll
  for (int c = 0; c < 4; ++c) oacc[c] = f32x16{};
  unsigned ka[8];
#pragma unroll
  for (int s = 0; s < 8; ++s) ka[s] = 256 * r + 16 * ((2 * s + hi) ^ fsw(r));
  unsigned va[4][2];
#pragma unroll
  for (int c = 0; c < 4; ++c)
#pragma unroll
    for (int t = 0; t < 2; ++t)
      va[c][t] = 16384 + 256 * (8 * t + 4 * hi + q4) + 16 * (4 * (c ^ q4) + (lo2 ^ (2 * t + hi))) + 8 * (p & 1);

  unsigned kslot = 8;
  for (int i = 0; i < 16; ++i) {
    BARRIER();
    STAMP(10 + i);
    unsigned kbase = lds0 + kslot * 16384;
    kslot += 2; if (kslot >= 10) kslot -= 10;
    f32x16 s0 = f32x16{}, s1 = f32x16{};
#pragma unroll
    for (int hf = 0; hf < 2; ++hf) {
      bf16x8 kf[4][2];
#pragma unroll
      for (int s = 0; s < 4; ++s) {
        kf[s][0] = lds_read16(kbase + ka[4 * hf + s]);
        kf[s][1] = lds_read16(kbase + ka[4 * hf + s] + 8192);
      }
#pragma unroll
      for (int s = 0; s < 4; ++s) {
        s0 = __builtin_amdgcn_mfma_f32_32x32x16_bf16(kf[s][0], qf[4 * hf + s], s0, 0, 0, 0);
        s1 = __builtin_amdgcn_mfma_f32_32x32x16_bf16(kf[s][1], qf[4 * hf + s], s1, 0, 0, 0);
      }
    }
    s16x4 vf[2][4][2];
#pragma unroll
    for (int ks = 0; ks < 2; ++ks)
#pragma unroll
      for (int c = 0; c < 4; ++c) {
        vf[ks][c][0] = tr_read(kbase + va[c][0] + ks * 4096);
        vf[ks][c][1] = tr_read(kbase + va[c][1] + ks * 4096);
      }
    float mx = s0[0];
#pragma unroll
    for (int j = 1; j < 16; ++j) mx = fmaxf(mx, s0[j]);
#pragma unroll
    for (int j = 0; j < 16; ++j) mx = fmaxf(mx, s1[j]);
    {
      auto rr = __builtin_amdgcn_permlane32_swap(__float_as_uint(mx), __float_as_uint(mx), false, false);
      mx = fmaxf(__uint_as_float(rr[0]), __uint_as_float(rr[1]));
    }
    float mnew = fmaxf(m, mx * cs);
    float alpha = __builtin_amdgcn_exp2f(m - mnew);
    m = mnew;
    if (__builtin_expect(__any(alpha != 1.f), 0)) {
#pragma unroll
      for (int c = 0; c < 4; ++c) oacc[c] *= alpha;
    }
    float ps = 0.f;
#pragma unroll
    for (int j = 0; j < 16; ++j) { s0[j] = __builtin_amdgcn_exp2f(fmaf(s0[j], cs, -mnew)); ps += s0[j]; }
    bf16x8 pb[2];
#pragma unroll
    for (int sp = 0; sp < 2; ++sp) {
      u32x4 u0 = {pk2(s0[8 * sp + 0], s0[8 * sp + 1]), pk2(s0[8 * sp + 2], s0[8 * sp + 3]),
                  pk2(s0[8 * sp + 4], s0[8 * sp + 5]), pk2(s0[8 * sp + 6], s0[8 * sp + 7])};
      pb[sp] = __builtin_bit_cast(bf16x8, u0);
    }
#pragma unroll
    for (int ks = 0; ks < 2; ++ks) {
#pragma unroll
      for (int c = 0; c < 4; ++c) {
        s16x4 a0 = vf[ks][c][0], a1 = vf[ks][c][1];
        bf16x8 a = {a0[0], a0[1], a0[2], a0[3], a1[0], a1[1], a1[2], a1[3]};
        oacc[c] = __builtin_amdgcn_mfma_f32_32x32x16_bf16(a, pb[ks], oacc[c], 0, 0, 0);
      }
    }
#pragma unroll
    for (int ks = 0; ks < 2; ++ks)
#pragma unroll
      for (int c = 0; c < 4; ++c) {
        vf[ks][c][0] = tr_read(kbase + va[c][0] + (2 + ks) * 4096);
        vf[ks][c][1] = tr_read(kbase + va[c][1] + (2 + ks) * 4096);
      }
#pragma unroll
    for (int j = 0; j < 16; ++j) { s1[j] = __builtin_amdgcn_exp2f(fmaf(s1[j], cs, -mnew)); ps += s1[j]; }
    l = l * alpha + ps;
#pragma unroll
    for (int sp = 0; sp < 2; ++sp) {
      u32x4 u1 = {pk2(s1[8 * sp + 0], s1[8 * sp + 1]), pk2(s1[8 * sp + 2], s1[8 * sp + 3]),
                  pk2(s1[8 * sp + 4], s1[8 * sp + 5]), pk2(s1[8 * sp + 6], s1[8 * sp + 7])};
      pb[sp] = __builtin_bit_cast(bf16x8, u1);
    }
#pragma unroll
    for (int ks = 0; ks < 2; ++ks) {
#pragma unroll
      for (int c = 0; c < 4; ++c) {
        s16x4 a0 = vf[ks][c][0], a1 = vf[ks][c][1];
        bf16x8 a = {a0[0], a0[1], a0[2], a0[3], a1[0], a1[1], a1[2], a1[3]};
        oacc[c] = __builtin_amdgcn_mfma_f32_32x32x16_bf16(a, pb[ks], oacc[c], 0, 0, 0);
      }
    }
  }
  STAMP(26);
  {
    auto rr = __builtin_amdgcn_permlane32_swap(__float_as_uint(l), __float_as_uint(l), false, false);
    float lt = __uint_as_float(rr[0]) + __uint_as_float(rr[1]);
    float inv = 1.f / lt;
    if (qrow < SQ) {
      char* ob = (char*)OT + ((size_t)((7 * b + (qrow >> 4)) * 128 + 4 * h)) * 1024 + (qrow & 15) * 16 + 8 * hi;
#pragma unroll
      for (int c = 0; c < 4; ++c)
#pragma unroll
        for (int g = 0; g < 4; ++g) {
          u32x2 v = {pk2(oacc[c][4 * g + 0] * inv, oacc[c][4 * g + 1] * inv),
                     pk2(oacc[c][4 * g + 2] * inv, oacc[c][4 * g + 3] * inv)};
          *(u32x2*)(ob + c * 1024 + g * 256) = v;
        }
    }
  }
}

constexpr unsigned G2_LDS = 6 * 24576;
__global__ __launch_bounds__(384, 1) void oproj_kernel(const short* __restrict__ OT, const short* __restrict__ WT,
                                                       short* __restrict__ out) {
  extern __shared__ __attribute__((aligned(16))) char smem[];
  const unsigned lds0 = (unsigned)(uintptr_t)smem;
  const int tid = threadIdx.x;
  const int w = __builtin_amdgcn_readfirstlane(tid >> 6);
  const int lane = tid & 63;
  const int bid = blockIdx.x;
  const int mt = bid % 14, nt = bid / 14;
  if (w >= 4) {
    const int L = w - 4;
    const char* Ab = (const char*)OT + ((size_t)((mt * 4 + 2 * L) * 128)) * 1024 + lane * 16;
    const char* Bb = (const char*)WT + ((size_t)((nt * 2 + L) * 128)) * 1024 + lane * 16;
    auto stage = [&](int st, unsigned slot) {
      unsigned base = lds0 + slot * 24576;
#pragma unroll
      for (int mb = 0; mb < 2; ++mb)
#pragma unroll
        for (int k = 0; k < 4; ++k)
          glds16<false>(Ab + (size_t)(mb * 128 + st * 4 + k) * 1024, base + (2 * L + mb) * 4096 + k * 1024);
#pragma unroll
      for (int k = 0; k < 4; ++k) glds16<false>(Bb + (st * 4 + k) * 1024, base + 16384 + L * 4096 + k * 1024);
    };
    stage(0, 0); stage(1, 1); stage(2, 2); stage(3, 3); stage(4, 4);
    unsigned slot = 5;
    for (int st = 0; st < 32; ++st) {
      if (st <= 27) WAITV(48);
      else if (st == 28) WAITV(36);
      else if (st == 29) WAITV(24);
      else if (st == 30) WAITV(12);
      else WAITV(0);
      __builtin_amdgcn_s_barrier();
      if (st + 5 < 32) { stage(st + 5, slot); slot = slot + 1; if (slot >= 6) slot = 0; }
    }
    return;
  }
  f32x4 acc0 = f32x4{}, acc1 = f32x4{};
  unsigned slot = 0;
  for (int st = 0; st < 32; ++st) {
    BARRIER();
    unsigned base = lds0 + slot * 24576 + lane * 16;
    bf16x8 a[4], b0[4], b1[4];
#pragma unroll
    for (int k = 0; k < 4; ++k) {
      a[k] = lds_read16(base + w * 4096 + k * 1024);
      b0[k] = lds_read16(base + 16384 + k * 1024);
      b1[k] = lds_read16(base + 16384 + 4096 + k * 1024);
    }
#pragma unroll
    for (int k = 0; k < 4; ++k) {
      acc0 = __builtin_amdgcn_mfma_f32_16x16x32_bf16(a[k], b0[k], acc0, 0, 0, 0);
      acc1 = __builtin_amdgcn_mfma_f32_16x16x32_bf16(a[k], b1[k], acc1, 0, 0, 0);
    }
    slot = slot + 1; if (slot >= 6) slot = 0;
  }
  const int g = lane >> 4, li = lane & 15;
  const int m0 = mt * 64 + 16 * w + 4 * g, n0 = nt * 32 + li;
#pragma unroll
  for (int j = 0; j < 4; ++j) {
    unsigned v0 = pk2(acc0[j], 0.f), v1 = pk2(acc1[j], 0.f);
    out[(size_t)(m0 + j) * 512 + n0] = (short)(v0 & 0xffff);
    out[(size_t)(m0 + j) * 512 + n0 + 16] = (short)(v1 & 0xffff);
  }
}

extern "C" void kernel_launch(void* const* d_in, const int* in_sizes, int n_in, void* d_out, int out_size,
                              void* d_ws, size_t ws_size, hipStream_t stream) {
  const short* x = (const short*)d_in[0];
  const short* kc = (const short*)d_in[1];
  const short* vc = (const short*)d_in[2];
  const short* wq = (const short*)d_in[3];
  const short* wo = (const short*)d_in[4];
  short* OT = (short*)d_ws;
  short* WT = (short*)((char*)d_ws + (size_t)8 * 1024 * 1024);
  (void)hipFuncSetAttribute((const void*)attn_kernel, hipFuncAttributeMaxDynamicSharedMemorySize, LDS_BYTES);
  (void)hipFuncSetAttribute((const void*)oproj_kernel, hipFuncAttributeMaxDynamicSharedMemorySize, G2_LDS);
  attn_kernel<<<256, 384, LDS_BYTES, stream>>>(x, kc, vc, wq, wo, OT, WT);
  oproj_kernel<<<224, 384, G2_LDS, stream>>>(OT, WT, (short*)d_out);
}
```

```cpp
#include <hip/hip_runtime.h>
#include <hip/hip_bf16.h>
#include <stdint.h>
#include <type_traits>

typedef short bf16x8 __attribute__((ext_vector_type(8)));
typedef short s16x4 __attribute__((ext_vector_type(4)));
typedef float f32x16 __attribute__((ext_vector_type(16)));
typedef float f32x4 __attribute__((ext_vector_type(4)));
typedef float f32x2 __attribute__((ext_vector_type(2)));
typedef unsigned u32x4 __attribute__((ext_vector_type(4)));
typedef unsigned u32x2 __attribute__((ext_vector_type(2)));
typedef __bf16 bf16x2_t __attribute__((ext_vector_type(2)));

constexpr int NB = 8, SQ = 112, DM = 512, NH = 32, SK = 1024, DH = 128;
constexpr int HD = NH * DH;

#define LDS_AS __attribute__((address_space(3)))

__device__ __forceinline__ unsigned pk2(float a, float b) {
  f32x2 v = {a, b};
  bf16x2_t r = __builtin_convertvector(v, bf16x2_t);
  return __builtin_bit_cast(unsigned, r);
}

__device__ __forceinline__ unsigned fsw(unsigned row) { return ((row & 3) << 2) | ((row >> 2) & 3); }

__device__ __forceinline__ s16x4 tr_read(unsigned lds_byte_addr) {
  LDS_AS s16x4* p = (LDS_AS s16x4*)(uintptr_t)lds_byte_addr;
  return __builtin_amdgcn_ds_read_tr16_b64_v4i16(p);
}
__device__ __forceinline__ bf16x8 lds_read16(unsigned lds_byte_addr) {
  LDS_AS bf16x8* p = (LDS_AS bf16x8*)(uintptr_t)lds_byte_addr;
  return *p;
}

template <bool NT>
__device__ __forceinline__ void glds16(const void* gsrc, unsigned lds_dst_uniform) {
  unsigned keep;
  unsigned dst = __builtin_amdgcn_readfirstlane(lds_dst_uniform);
  if constexpr (NT)
    asm volatile("s_mov_b32 %0, m0\n\ts_mov_b32 m0, %2\n\ts_nop 0\n\tglobal_load_lds_dwordx4 %1, off nt\n\ts_mov_b32 m0, %0"
                 : "=&s"(keep) : "v"(gsrc), "s"(dst) : "memory");
  else
    asm volatile("s_mov_b32 %0, m0\n\ts_mov_b32 m0, %2\n\ts_nop 0\n\tglobal_load_lds_dwordx4 %1, off\n\ts_mov_b32 m0, %0"
                 : "=&s"(keep) : "v"(gsrc), "s"(dst) : "memory");
}

#define WAITV(n) asm volatile("s_waitcnt vmcnt(" #n ")" ::: "memory")

constexpr unsigned LDS_BYTES = 163840;
#define BARRIER() do { asm volatile("s_waitcnt lgkmcnt(0)" ::: "memory"); __builtin_amdgcn_s_barrier(); } while (0)

__global__ __launch_bounds__(384, 1) void attn_kernel(const short* __restrict__ x, const short* __restrict__ kc,
                                                      const short* __restrict__ vc, const short* __restrict__ wq,
                                                      const short* __restrict__ wo, short* __restrict__ OT,
                                                      short* __restrict__ WT, unsigned* __restrict__ cnt,
                                                      short* __restrict__ out) {
  extern __shared__ __attribute__((aligned(16))) char smem[];
  const unsigned lds0 = (unsigned)(uintptr_t)smem;
  const int tid = threadIdx.x;
  const int w = __builtin_amdgcn_readfirstlane(tid >> 6);
  const int lane = tid & 63;
  const int r = lane & 31, hi = lane >> 5;
  const int bid = blockIdx.x;
#define STAMP(i)
  STAMP(0);
  const int h = (bid & 7) * 4 + ((bid >> 3) & 3);
  const int b = bid >> 5;

  if (w >= 4) {
    const int L = w - 4;
    const char* Kh = (const char*)(kc + ((size_t)(b * NH + h)) * SK * DH);
    const char* Vh = (const char*)(vc + ((size_t)(b * NH + h)) * SK * DH);
    const char* Wh = (const char*)(wq + h * DH);
    const char* Xb = (const char*)(x + (size_t)b * SQ * DM);
    const unsigned lrow = lane >> 4;
    const char* Wo = (const char*)(wo + (size_t)((bid >> 1) * 32) * 512 + (bid & 1) * 256);
    unsigned so_kv[8], so_w[8], so_x[8], so_o[8];
#pragma unroll
    for (int j = 0; j < 8; ++j) {
      unsigned row = 32 * L + 4 * j + lrow;
      unsigned ch = (lane & 15) ^ ((lrow << 2) | (j & 3));
      so_kv[j] = row * 256 + ch * 16;
      so_w[j] = row * (HD * 2) + ch * 16;
      unsigned xrow = 64 * L + 8 * j + (lane >> 3);
      unsigned xch = (lane & 7) ^ ((xrow >> 1) & 7);
      unsigned xrc = xrow < SQ ? xrow : SQ - 1;
      so_x[j] = xrc * (DM * 2) + xch * 16;
      unsigned orow = 16 * L + 2 * j + (lane >> 5);
      unsigned och = (lane & 31) ^ (((orow & 3) << 2) | ((orow >> 2) & 3));
      so_o[j] = orow * 1024 + och * 16;
    }
    auto dma_item = [&](int n) {
      unsigned slot = (unsigned)n % 10u;
      unsigned dst = lds0 + slot * 16384 + L * 8192;
      if (n < 16) {
        int t = n >> 1;
        if (n & 1) {
          const char* g = Wh + (size_t)t * 64 * (HD * 2);
#pragma unroll
          for (int j = 0; j < 8; ++j) glds16<false>(g + so_w[j], dst + j * 1024);
        } else {
          const char* g = Xb + t * 128;
#pragma unroll
          for (int j = 0; j < 8; ++j) glds16<false>(g + so_x[j], dst + j * 1024);
        }
      } else if (n == 16) {
#pragma unroll
        for (int j = 0; j < 8; ++j) glds16<false>(Wo + so_o[j], dst + j * 1024);
      } else if (n >= 18) {
        int t = (n - 18) >> 1;
        const char* g = ((n & 1) ? Vh : Kh) + (size_t)t * 16384;
#pragma unroll
        for (int j = 0; j < 8; ++j) glds16<true>(g + so_kv[j], dst + j * 1024);
      }
    };
    for (int n = 0; n < 7; ++n) dma_item(n);
    WAITV(32);
    for (int n = 7; n < 10; ++n) dma_item(n);
    for (int st = 0; st < 25; ++st) {
      if (st == 0) WAITV(56);
      else if (st >= 5 && st <= 7) WAITV(40);
      else if (st == 9 || st == 22) WAITV(32);
      else if (st == 23) WAITV(16);
      else if (st == 24) WAITV(0);
      else WAITV(48);
      __builtin_amdgcn_s_barrier();
      if (st == 9) { dma_item(24); dma_item(25); dma_item(26); dma_item(27); }
      else if (st >= 1 && st <= 20 && st != 8) { dma_item(2 * st + 8); dma_item(2 * st + 9); }
    }
    __builtin_amdgcn_s_barrier();
    {
      unsigned spins = 0;
      while (__hip_atomic_load(cnt, __ATOMIC_RELAXED, __HIP_MEMORY_SCOPE_AGENT) < 256u) {
        __builtin_amdgcn_s_sleep(2);
        if (++spins > (1u << 24)) break;
      }
      __builtin_amdgcn_fence(__ATOMIC_ACQUIRE, "agent");
      asm volatile("s_waitcnt vmcnt(0)" ::: "memory");
    }
    {
      const int mh = h & 1, nt = h >> 1;
      const char* Ab = (const char*)OT + ((size_t)((7 * b + 3 * mh + 2 * L) * 128)) * 1024 + lane * 16;
      const char* Bb = (const char*)WT + ((size_t)((nt * 2 + L) * 128)) * 1024 + lane * 16;
      auto stage = [&](int st, unsigned slot) {
        unsigned base = lds0 + slot * 24576;
#pragma unroll
        for (int mb = 0; mb < 2; ++mb)
#pragma unroll
          for (int k = 0; k < 4; ++k)
            glds16<false>(Ab + (size_t)(mb * 128 + st * 4 + k) * 1024, base + (2 * L + mb) * 4096 + k * 1024);
#pragma unroll
        for (int k = 0; k < 4; ++k) glds16<false>(Bb + (st * 4 + k) * 1024, base + 16384 + L * 4096 + k * 1024);
      };
      stage(0, 0); stage(1, 1); stage(2, 2); stage(3, 3); stage(4, 4);
      unsigned slot = 5;
      for (int st = 0; st < 32; ++st) {
        if (st <= 27) WAITV(48);
        else if (st == 28) WAITV(36);
        else if (st == 29) WAITV(24);
        else if (st == 30) WAITV(12);
        else WAITV(0);
        __builtin_amdgcn_s_barrier();
        if (st + 5 < 32) { stage(st + 5, slot); slot = slot + 1; if (slot >= 6) slot = 0; }
      }
    }
    return;
  }

  const unsigned blk = (lane >> 4) & 1, q4 = (lane & 15) >> 2, p = lane & 3;
  const unsigned lo2 = 2 * blk + (p >> 1);
  const int qrow = 32 * w + r;

  f32x16 qacc[4];
#pragma unroll
  for (int c = 0; c < 4; ++c) qacc[c] = f32x16{};
  {
    unsigned wa[4][2];
#pragma unroll
    for (int c = 0; c < 4; ++c)
#pragma unroll
      for (int t = 0; t < 2; ++t)
        wa[c][t] = 16384 + 256 * (8 * hi + 4 * t + q4) + 16 * (4 * (c ^ q4) + (lo2 ^ (2 * hi + t))) + 8 * (p & 1);
    unsigned xa[4];
#pragma unroll
    for (int ks = 0; ks < 4; ++ks) xa[ks] = qrow * 128 + 16 * ((2 * ks + hi) ^ ((qrow >> 1) & 7));
    unsigned xslot = 0;
    for (int t8 = 0; t8 < 8; ++t8) {
      BARRIER();
      STAMP(1 + t8);
      unsigned xbase = lds0 + xslot * 16384;
      xslot += 2; if (xslot >= 10) xslot -= 10;
      bf16x8 xf[4];
      s16x4 af[4][4][2];
#pragma unroll
      for (int ks = 0; ks < 4; ++ks) xf[ks] = lds_read16(xbase + xa[ks]);
#pragma unroll
      for (int ks = 0; ks < 4; ++ks)
#pragma unroll
        for (int c = 0; c < 4; ++c) {
          af[ks][c][0] = tr_read(xbase + wa[c][0] + ks * 4096);
          af[ks][c][1] = tr_read(xbase + wa[c][1] + ks * 4096);
        }
#pragma unroll
      for (int ks = 0; ks < 4; ++ks) {
#pragma unroll
        for (int c = 0; c < 4; ++c) {
          s16x4 a0 = af[ks][c][0], a1 = af[ks][c][1];
          bf16x8 a = {a0[0], a0[1], a0[2], a0[3], a1[0], a1[1], a1[2], a1[3]};
          qacc[c] = __builtin_amdgcn_mfma_f32_32x32x16_bf16(a, xf[ks], qacc[c], 0, 0, 0);
        }
      }
    }
  }
  BARRIER();
  STAMP(9);
  bf16x8 qf[8];
  {
    unsigned qs = lds0 + 4 * 16384 + w * 8192;
#pragma unroll
    for (int c = 0; c < 4; ++c)
#pragma unroll
      for (int g = 0; g < 4; ++g) {
        u32x2 v = {pk2(qacc[c][4 * g + 0], qacc[c][4 * g + 1]), pk2(qacc[c][4 * g + 2], qacc[c][4 * g + 3])};
        unsigned ch = 4 * c + g;
        unsigned a = qs + r * 256 + 16 * (ch ^ (r & 15)) + 8 * hi;
        *(LDS_AS u32x2*)(uintptr_t)a = v;
      }
    {
      const unsigned g = lane >> 4;
      char* dst = (char*)WT + ((size_t)(((bid & 1) * 16 + 4 * w) * 128 + (bid >> 1))) * 1024 + lane * 16;
#pragma unroll
      for (int nt = 0; nt < 4; ++nt) {
        s16x4 t01[2];
#pragma unroll
        for (int t = 0; t < 2; ++t) {
          unsigned row = 8 * g + 4 * t + q4;
          unsigned ch = 2 * (4 * w + nt) + (p >> 1);
          ch ^= ((row & 3) << 2) | ((row >> 2) & 3);
          t01[t] = tr_read(lds0 + 6 * 16384 + row * 512 + ch * 16 + 8 * (p & 1));
        }
        bf16x8 o = {t01[0][0], t01[0][1], t01[0][2], t01[0][3], t01[1][0], t01[1][1], t01[1][2], t01[1][3]};
        asm volatile("global_store_dwordx4 %0, %1, off sc1\n\ts_nop 1" :: "v"(dst + (size_t)nt * 128 * 1024), "v"(o) : "memory");
      }
    }
    asm volatile("s_waitcnt lgkmcnt(0)" ::: "memory");
#pragma unroll
    for (int s = 0; s < 8; ++s) {
      unsigned ch = 2 * s + hi;
      qf[s] = lds_read16(qs + r * 256 + 16 * (ch ^ (r & 15)));
    }
  }

  const float cs = 0.08838834764831845f * 1.4426950408889634f;
  float m = -__builtin_inff(), l = 0.f;
  f32x16 oacc[4];
#pragma unroll
  for (int c = 0; c < 4; ++c) oacc[c] = f32x16{};
  unsigned ka[8];
#pragma unroll
  for (int s = 0; s < 8; ++s) ka[s] = 256 * r + 16 * ((2 * s + hi) ^ fsw(r));
  unsigned va[4][2];
#pragma unroll
  for (int c = 0; c < 4; ++c)
#pragma unroll
    for (int t = 0; t < 2; ++t)
      va[c][t] = 16384 + 256 * (8 * t + 4 * hi + q4) + 16 * (4 * (c ^ q4) + (lo2 ^ (2 * t + hi))) + 8 * (p & 1);

  unsigned kslot = 8;
  for (int i = 0; i < 16; ++i) {
    BARRIER();
    STAMP(10 + i);
    unsigned kbase = lds0 + kslot * 16384;
    kslot += 2; if (kslot >= 10) kslot -= 10;
    f32x16 s0 = f32x16{}, s1 = f32x16{};
#pragma unroll
    for (int hf = 0; hf < 2; ++hf) {
      bf16x8 kf[4][2];
#pragma unroll
      for (int s = 0; s < 4; ++s) {
        kf[s][0] = lds_read16(kbase + ka[4 * hf + s]);
        kf[s][1] = lds_read16(kbase + ka[4 * hf + s] + 8192);
      }
#pragma unroll
      for (int s = 0; s < 4; ++s) {
        s0 = __builtin_amdgcn_mfma_f32_32x32x16_bf16(kf[s][0], qf[4 * hf + s], s0, 0, 0, 0);
        s1 = __builtin_amdgcn_mfma_f32_32x32x16_bf16(kf[s][1], qf[4 * hf + s], s1, 0, 0, 0);
      }
    }
    s16x4 vf[2][4][2];
#pragma unroll
    for (int ks = 0; ks < 2; ++ks)
#pragma unroll
      for (int c = 0; c < 4; ++c) {
        vf[ks][c][0] = tr_read(kbase + va[c][0] + ks * 4096);
        vf[ks][c][1] = tr_read(kbase + va[c][1] + ks * 4096);
      }
    float mx = s0[0];
#pragma unroll
    for (int j = 1; j < 16; ++j) mx = fmaxf(mx, s0[j]);
#pragma unroll
    for (int j = 0; j < 16; ++j) mx = fmaxf(mx, s1[j]);
    {
      auto rr = __builtin_amdgcn_permlane32_swap(__float_as_uint(mx), __float_as_uint(mx), false, false);
      mx = fmaxf(__uint_as_float(rr[0]), __uint_as_float(rr[1]));
    }
    float mnew = fmaxf(m, mx * cs);
    float alpha = __builtin_amdgcn_exp2f(m - mnew);
    m = mnew;
    if (__builtin_expect(__any(alpha != 1.f), 0)) {
#pragma unroll
      for (int c = 0; c < 4; ++c) oacc[c] *= alpha;
    }
    float ps = 0.f;
#pragma unroll
    for (int j = 0; j < 16; ++j) { s0[j] = __builtin_amdgcn_exp2f(fmaf(s0[j], cs, -mnew)); ps += s0[j]; }
    bf16x8 pb[2];
#pragma unroll
    for (int sp = 0; sp < 2; ++sp) {
      u32x4 u0 = {pk2(s0[8 * sp + 0], s0[8 * sp + 1]), pk2(s0[8 * sp + 2], s0[8 * sp + 3]),
                  pk2(s0[8 * sp + 4], s0[8 * sp + 5]), pk2(s0[8 * sp + 6], s0[8 * sp + 7])};
      pb[sp] = __builtin_bit_cast(bf16x8, u0);
    }
#pragma unroll
    for (int ks = 0; ks < 2; ++ks) {
#pragma unroll
      for (int c = 0; c < 4; ++c) {
        s16x4 a0 = vf[ks][c][0], a1 = vf[ks][c][1];
        bf16x8 a = {a0[0], a0[1], a0[2], a0[3], a1[0], a1[1], a1[2], a1[3]};
        oacc[c] = __builtin_amdgcn_mfma_f32_32x32x16_bf16(a, pb[ks], oacc[c], 0, 0, 0);
      }
    }
#pragma unroll
    for (int ks = 0; ks < 2; ++ks)
#pragma unroll
      for (int c = 0; c < 4; ++c) {
        vf[ks][c][0] = tr_read(kbase + va[c][0] + (2 + ks) * 4096);
        vf[ks][c][1] = tr_read(kbase + va[c][1] + (2 + ks) * 4096);
      }
#pragma unroll
    for (int j = 0; j < 16; ++j) { s1[j] = __builtin_amdgcn_exp2f(fmaf(s1[j], cs, -mnew)); ps += s1[j]; }
    l = l * alpha + ps;
#pragma unroll
    for (int sp = 0; sp < 2; ++sp) {
      u32x4 u1 = {pk2(s1[8 * sp + 0], s1[8 * sp + 1]), pk2(s1[8 * sp + 2], s1[8 * sp + 3]),
                  pk2(s1[8 * sp + 4], s1[8 * sp + 5]), pk2(s1[8 * sp + 6], s1[8 * sp + 7])};
      pb[sp] = __builtin_bit_cast(bf16x8, u1);
    }
#pragma unroll
    for (int ks = 0; ks < 2; ++ks) {
#pragma unroll
      for (int c = 0; c < 4; ++c) {
        s16x4 a0 = vf[ks][c][0], a1 = vf[ks][c][1];
        bf16x8 a = {a0[0], a0[1], a0[2], a0[3], a1[0], a1[1], a1[2], a1[3]};
        oacc[c] = __builtin_amdgcn_mfma_f32_32x32x16_bf16(a, pb[ks], oacc[c], 0, 0, 0);
      }
    }
  }
  STAMP(26);
  {
    auto rr = __builtin_amdgcn_permlane32_swap(__float_as_uint(l), __float_as_uint(l), false, false);
    float lt = __uint_as_float(rr[0]) + __uint_as_float(rr[1]);
    float inv = 1.f / lt;
    if (qrow < SQ) {
      char* ob = (char*)OT + ((size_t)((7 * b + (qrow >> 4)) * 128 + 4 * h)) * 1024 + (qrow & 15) * 16 + 8 * hi;
#pragma unroll
      for (int c = 0; c < 4; ++c)
#pragma unroll
        for (int g = 0; g < 4; ++g) {
          u32x2 v = {pk2(oacc[c][4 * g + 0] * inv, oacc[c][4 * g + 1] * inv),
                     pk2(oacc[c][4 * g + 2] * inv, oacc[c][4 * g + 3] * inv)};
          asm volatile("global_store_dwordx2 %0, %1, off sc1\n\ts_nop 1" :: "v"(ob + c * 1024 + g * 256), "v"(v) : "memory");
        }
    }
  }
  asm volatile("s_waitcnt vmcnt(0)" ::: "memory");
  BARRIER();
  if (tid == 0) __hip_atomic_fetch_add(cnt, 1u, __ATOMIC_RELAXED, __HIP_MEMORY_SCOPE_AGENT);
  STAMP(27);
  {
    const unsigned g = lane >> 4, li = lane & 15;
    f32x4 acc0 = f32x4{}, acc1 = f32x4{};
    unsigned slot = 0;
    for (int st = 0; st < 32; ++st) {
      BARRIER();
      unsigned base = lds0 + slot * 24576 + lane * 16;
      bf16x8 a[4], b0[4], b1[4];
#pragma unroll
      for (int k = 0; k < 4; ++k) {
        a[k] = lds_read16(base + w * 4096 + k * 1024);
        b0[k] = lds_read16(base + 16384 + k * 1024);
        b1[k] = lds_read16(base + 16384 + 4096 + k * 1024);
      }
#pragma unroll
      for (int k = 0; k < 4; ++k) {
        acc0 = __builtin_amdgcn_mfma_f32_16x16x32_bf16(a[k], b0[k], acc0, 0, 0, 0);
        acc1 = __builtin_amdgcn_mfma_f32_16x16x32_bf16(a[k], b1[k], acc1, 0, 0, 0);
      }
      slot = slot + 1; if (slot >= 6) slot = 0;
    }
    const int mh = h & 1, nt = h >> 1;
    const int trow = 16 * w + 4 * (int)g;
    const int row0 = 48 * mh + trow;
#pragma unroll
    for (int j = 0; j < 4; ++j) {
      int tr = trow + j;
      bool ok = mh ? (tr >= 8) : (tr < 56);
      if (ok) {
        unsigned v0 = pk2(acc0[j], 0.f), v1 = pk2(acc1[j], 0.f);
        short* o = out + (size_t)(b * SQ + row0 + j) * 512 + nt * 32 + li;
        o[0] = (short)(v0 & 0xffff);
        o[16] = (short)(v1 & 0xffff);
      }
    }
  }
}

extern "C" void kernel_launch(void* const* d_in, const int* in_sizes, int n_in, void* d_out, int out_size,
                              void* d_ws, size_t ws_size, hipStream_t stream) {
  const short* x = (const short*)d_in[0];
  const short* kc = (const short*)d_in[1];
  const short* vc = (const short*)d_in[2];
  const short* wq = (const short*)d_in[3];
  const short* wo = (const short*)d_in[4];
  unsigned* cnt = (unsigned*)d_ws;
  short* OT = (short*)((char*)d_ws + 4096);
  short* WT = (short*)((char*)d_ws + 4096 + (size_t)8 * 1024 * 1024);
  (void)hipFuncSetAttribute((const void*)attn_kernel, hipFuncAttributeMaxDynamicSharedMemorySize, LDS_BYTES);
  (void)hipMemsetAsync(cnt, 0, 512, stream);
  attn_kernel<<<256, 384, LDS_BYTES, stream>>>(x, kc, vc, wq, wo, OT, WT, cnt, (short*)d_out);
}
```

```cpp
#include <hip/hip_runtime.h>
#include <hip/hip_bf16.h>
#include <stdint.h>
#include <type_traits>

typedef short bf16x8 __attribute__((ext_vector_type(8)));
typedef short s16x4 __attribute__((ext_vector_type(4)));
typedef float f32x16 __attribute__((ext_vector_type(16)));
typedef float f32x4 __attribute__((ext_vector_type(4)));
typedef float f32x2 __attribute__((ext_vector_type(2)));
typedef unsigned u32x4 __attribute__((ext_vector_type(4)));
typedef unsigned u32x2 __attribute__((ext_vector_type(2)));
typedef __bf16 bf16x2_t __attribute__((ext_vector_type(2)));

constexpr int NB = 8, SQ = 112, DM = 512, NH = 32, SK = 1024, DH = 128;
constexpr int HD = NH * DH;

#define LDS_AS __attribute__((address_space(3)))

__device__ __forceinline__ unsigned pk2(float a, float b) {
  f32x2 v = {a, b};
  bf16x2_t r = __builtin_convertvector(v, bf16x2_t);
  return __builtin_bit_cast(unsigned, r);
}

__device__ __forceinline__ unsigned fsw(unsigned row) { return ((row & 3) << 2) | ((row >> 2) & 3); }

__device__ __forceinline__ s16x4 tr_read(unsigned lds_byte_addr) {
  LDS_AS s16x4* p = (LDS_AS s16x4*)(uintptr_t)lds_byte_addr;
  return __builtin_amdgcn_ds_read_tr16_b64_v4i16(p);
}
__device__ __forceinline__ bf16x8 lds_read16(unsigned lds_byte_addr) {
  LDS_AS bf16x8* p = (LDS_AS bf16x8*)(uintptr_t)lds_byte_addr;
  return *p;
}

template <bool NT>
__device__ __forceinline__ void glds16(const void* gsrc, unsigned lds_dst_uniform) {
  unsigned keep;
  unsigned dst = __builtin_amdgcn_readfirstlane(lds_dst_uniform);
  if constexpr (NT)
    asm volatile("s_mov_b32 %0, m0\n\ts_mov_b32 m0, %2\n\ts_nop 0\n\tglobal_load_lds_dwordx4 %1, off nt\n\ts_mov_b32 m0, %0"
                 : "=&s"(keep) : "v"(gsrc), "s"(dst) : "memory");
  else
    asm volatile("s_mov_b32 %0, m0\n\ts_mov_b32 m0, %2\n\ts_nop 0\n\tglobal_load_lds_dwordx4 %1, off\n\ts_mov_b32 m0, %0"
                 : "=&s"(keep) : "v"(gsrc), "s"(dst) : "memory");
}

#define WAITV(n) asm volatile("s_waitcnt vmcnt(" #n ")" ::: "memory")

constexpr unsigned LDS_BYTES = 163840;
#define BARRIER() do { asm volatile("s_waitcnt lgkmcnt(0)" ::: "memory"); __builtin_amdgcn_s_barrier(); } while (0)

__global__ __launch_bounds__(384, 1) void attn_kernel(const short* __restrict__ x, const short* __restrict__ kc,
                                                      const short* __restrict__ vc, const short* __restrict__ wq,
                                                      const short* __restrict__ wo, short* __restrict__ OT,
                                                      short* __restrict__ WT) {
  extern __shared__ __attribute__((aligned(16))) char smem[];
  const unsigned lds0 = (unsigned)(uintptr_t)smem;
  const int tid = threadIdx.x;
  const int w = __builtin_amdgcn_readfirstlane(tid >> 6);
  const int lane = tid & 63;
  const int r = lane & 31, hi = lane >> 5;
  const int bid = blockIdx.x;
#define STAMP(i)
  STAMP(0);
  const int h = (bid & 7) * 4 + ((bid >> 3) & 3);
  const int b = bid >> 5;

  if (w >= 4) {
    const int L = w - 4;
    const char* Kh = (const char*)(kc + ((size_t)(b * NH + h)) * SK * DH);
    const char* Vh = (const char*)(vc + ((size_t)(b * NH + h)) * SK * DH);
    const char* Wh = (const char*)(wq + h * DH);
    const char* Xb = (const char*)(x + (size_t)b * SQ * DM);
    const char* Wo = (const char*)(wo + (size_t)((bid >> 1) * 32) * 512 + (bid & 1) * 256);
    const unsigned lrow = lane >> 4;
    unsigned so_kv[8], so_w[8], so_x[8], so_o[8];
#pragma unroll
    for (int j = 0; j < 8; ++j) {
      unsigned row = 32 * L + 4 * j + lrow;
      unsigned ch = (lane & 15) ^ ((lrow << 2) | (j & 3));
      so_kv[j] = row * 256 + ch * 16;
      so_w[j] = row * (HD * 2) + ch * 16;
      unsigned xrow = 64 * L + 8 * j + (lane >> 3);
      unsigned xch = (lane & 7) ^ ((xrow >> 1) & 7);
      unsigned xrc = xrow < SQ ? xrow : SQ - 1;
      so_x[j] = xrc * (DM * 2) + xch * 16;
      unsigned orow = 16 * L + 2 * j + (lane >> 5);
      unsigned och = (lane & 31) ^ (((orow & 3) << 2) | ((orow >> 2) & 3));
      so_o[j] = orow * 1024 + och * 16;
    }
    auto dma_item = [&](int n) {
      unsigned slot = (unsigned)n % 10u;
      unsigned dst = lds0 + slot * 16384 + L * 8192;
      if (n < 16) {
        int t = n >> 1;
        if (n & 1) {
          const char* g = Wh + (size_t)t * 64 * (HD * 2);
#pragma unroll
          for (int j = 0; j < 8; ++j) glds16<false>(g + so_w[j], dst + j * 1024);
        } else {
          const char* g = Xb + t * 128;
#pragma unroll
          for (int j = 0; j < 8; ++j) glds16<false>(g + so_x[j], dst + j * 1024);
        }
      } else if (n == 16) {
#pragma unroll
        for (int j = 0; j < 8; ++j) glds16<false>(Wo + so_o[j], dst + j * 1024);
      } else if (n >= 18) {
        int t = (n - 18) >> 1;
        const char* g = ((n & 1) ? Vh : Kh) + (size_t)t * 16384;
#pragma unroll
        for (int j = 0; j < 8; ++j) glds16<true>(g + so_kv[j], dst + j * 1024);
      }
    };
    for (int n = 0; n < 7; ++n) dma_item(n);
    WAITV(32);
    for (int n = 7; n < 10; ++n) dma_item(n);
    for (int st = 0; st < 25; ++st) {
      if (st == 0) WAITV(56);
      else if (st >= 5 && st <= 7) WAITV(40);
      else if (st == 9 || st == 22) WAITV(32);
      else if (st == 23) WAITV(16);
      else if (st == 24) WAITV(0);
      else WAITV(48);
      __builtin_amdgcn_s_barrier();
      if (st == 9) { dma_item(24); dma_item(25); dma_item(26); dma_item(27); }
      else if (st >= 1 && st <= 20 && st != 8) { dma_item(2 * st + 8); dma_item(2 * st + 9); }
    }
    return;
  }

  const unsigned blk = (lane >> 4) & 1, q4 = (lane & 15) >> 2, p = lane & 3;
  const unsigned lo2 = 2 * blk + (p >> 1);
  const int qrow = 32 * w + r;

  f32x16 qacc[4];
#pragma unroll
  for (int c = 0; c < 4; ++c) qacc[c] = f32x16{};
  {
    unsigned wa[4][2];
#pragma unroll
    for (int c = 0; c < 4; ++c)
#pragma unroll
      for (int t = 0; t < 2; ++t)
        wa[c][t] = 16384 + 256 * (8 * hi + 4 * t + q4) + 16 * (4 * (c ^ q4) + (lo2 ^ (2 * hi + t))) + 8 * (p & 1);
    unsigned xa[4];
#pragma unroll
    for (int ks = 0; ks < 4; ++ks) xa[ks] = qrow * 128 + 16 * ((2 * ks + hi) ^ ((qrow >> 1) & 7));
    unsigned xslot = 0;
    for (int t8 = 0; t8 < 8; ++t8) {
      BARRIER();
      STAMP(1 + t8);
      unsigned xbase = lds0 + xslot * 16384;
      xslot += 2; if (xslot >= 10) xslot -= 10;
      bf16x8 xf[4];
      s16x4 af[4][4][2];
#pragma unroll
      for (int ks = 0; ks < 4; ++ks) xf[ks] = lds_read16(xbase + xa[ks]);
#pragma unroll
      for (int ks = 0; ks < 4; ++ks)
#pragma unroll
        for (int c = 0; c < 4; ++c) {
          af[ks][c][0] = tr_read(xbase + wa[c][0] + ks * 4096);
          af[ks][c][1] = tr_read(xbase + wa[c][1] + ks * 4096);
        }
#pragma unroll
      for (int ks = 0; ks < 4; ++ks) {
#pragma unroll
        for (int c = 0; c < 4; ++c) {
          s16x4 a0 = af[ks][c][0], a1 = af[ks][c][1];
          bf16x8 a = {a0[0], a0[1], a0[2], a0[3], a1[0], a1[1], a1[2], a1[3]};
          qacc[c] = __builtin_amdgcn_mfma_f32_32x32x16_bf16(a, xf[ks], qacc[c], 0, 0, 0);
        }
      }
    }
  }
  BARRIER();
  STAMP(9);
  bf16x8 qf[8];
  {
    unsigned qs = lds0 + 4 * 16384 + w * 8192;
#pragma unroll
    for (int c = 0; c < 4; ++c)
#pragma unroll
      for (int g = 0; g < 4; ++g) {
        u32x2 v = {pk2(qacc[c][4 * g + 0], qacc[c][4 * g + 1]), pk2(qacc[c][4 * g + 2], qacc[c][4 * g + 3])};
        unsigned ch = 4 * c + g;
        unsigned a = qs + r * 256 + 16 * (ch ^ (r & 15)) + 8 * hi;
        *(LDS_AS u32x2*)(uintptr_t)a = v;
      }
    {
      const unsigned g = lane >> 4;
      char* dst = (char*)WT + ((size_t)(((bid & 1) * 16 + 4 * w) * 128 + (bid >> 1))) * 1024 + lane * 16;
#pragma unroll
      for (int nt = 0; nt < 4; ++nt) {
        s16x4 t01[2];
#pragma unroll
        for (int t = 0; t < 2; ++t) {
          unsigned row = 8 * g + 4 * t + q4;
          unsigned ch = 2 * (4 * w + nt) + (p >> 1);
          ch ^= ((row & 3) << 2) | ((row >> 2) & 3);
          t01[t] = tr_read(lds0 + 6 * 16384 + row * 512 + ch * 16 + 8 * (p & 1));
        }
        bf16x8 o = {t01[0][0], t01[0][1], t01[0][2], t01[0][3], t01[1][0], t01[1][1], t01[1][2], t01[1][3]};
        *(bf16x8*)(dst + (size_t)nt * 128 * 1024) = o;
      }
    }
    asm volatile("s_waitcnt lgkmcnt(0)" ::: "memory");
#pragma unroll
    for (int s = 0; s < 8; ++s) {
      unsigned ch = 2 * s + hi;
      qf[s] = lds_read16(qs + r * 256 + 16 * (ch ^ (r & 15)));
    }
  }

  const float cs = 0.08838834764831845f * 1.4426950408889634f;
  float m = -__builtin_inff(), l = 0.f;
  f32x16 oacc[4];
#pragma unroll
  for (int c = 0; c < 4; ++c) oacc[c] = f32x16{};
  unsigned ka[8];
#pragma unroll
  for (int s = 0; s < 8; ++s) ka[s] = 256 * r + 16 * ((2 * s + hi) ^ fsw(r));
  unsigned va[4][2];
#pragma unroll
  for (int c = 0; c < 4; ++c)
#pragma unroll
    for (int t = 0; t < 2; ++t)
      va[c][t] = 16384 + 256 * (8 * t + 4 * hi + q4) + 16 * (4 * (c ^ q4) + (lo2 ^ (2 * t + hi))) + 8 * (p & 1);

  unsigned kslot = 8;
  for (int i = 0; i < 16; ++i) {
    BARRIER();
    STAMP(10 + i);
    unsigned kbase = lds0 + kslot * 16384;
    kslot += 2; if (kslot >= 10) kslot -= 10;
    f32x16 s0 = f32x16{}, s1 = f32x16{};
#pragma unroll
    for (int hf = 0; hf < 2; ++hf) {
      bf16x8 kf[4][2];
#pragma unroll
      for (int s = 0; s < 4; ++s) {
        kf[s][0] = lds_read16(kbase + ka[4 * hf + s]);
        kf[s][1] = lds_read16(kbase + ka[4 * hf + s] + 8192);
      }
#pragma unroll
      for (int s = 0; s < 4; ++s) {
        s0 = __builtin_amdgcn_mfma_f32_32x32x16_bf16(kf[s][0], qf[4 * hf + s], s0, 0, 0, 0);
        s1 = __builtin_amdgcn_mfma_f32_32x32x16_bf16(kf[s][1], qf[4 * hf + s], s1, 0, 0, 0);
      }
    }
    s16x4 vf[2][4][2];
#pragma unroll
    for (int ks = 0; ks < 2; ++ks)
#pragma unroll
      for (int c = 0; c < 4; ++c) {
        vf[ks][c][0] = tr_read(kbase + va[c][0] + ks * 4096);
        vf[ks][c][1] = tr_read(kbase + va[c][1] + ks * 4096);
      }
    float mx = s0[0];
#pragma unroll
    for (int j = 1; j < 16; ++j) mx = fmaxf(mx, s0[j]);
#pragma unroll
    for (int j = 0; j < 16; ++j) mx = fmaxf(mx, s1[j]);
    {
      auto rr = __builtin_amdgcn_permlane32_swap(__float_as_uint(mx), __float_as_uint(mx), false, false);
      mx = fmaxf(__uint_as_float(rr[0]), __uint_as_float(rr[1]));
    }
    float mnew = fmaxf(m, mx * cs);
    float alpha = __builtin_amdgcn_exp2f(m - mnew);
    m = mnew;
    if (__builtin_expect(__any(alpha != 1.f), 0)) {
#pragma unroll
      for (int c = 0; c < 4; ++c) oacc[c] *= alpha;
    }
    float ps = 0.f;
#pragma unroll
    for (int j = 0; j < 16; ++j) { s0[j] = __builtin_amdgcn_exp2f(fmaf(s0[j], cs, -mnew)); ps += s0[j]; }
    bf16x8 pb[2];
#pragma unroll
    for (int sp = 0; sp < 2; ++sp) {
      u32x4 u0 = {pk2(s0[8 * sp + 0], s0[8 * sp + 1]), pk2(s0[8 * sp + 2], s0[8 * sp + 3]),
                  pk2(s0[8 * sp + 4], s0[8 * sp + 5]), pk2(s0[8 * sp + 6], s0[8 * sp + 7])};
      pb[sp] = __builtin_bit_cast(bf16x8, u0);
    }
#pragma unroll
    for (int ks = 0; ks < 2; ++ks) {
#pragma unroll
      for (int c = 0; c < 4; ++c) {
        s16x4 a0 = vf[ks][c][0], a1 = vf[ks][c][1];
        bf16x8 a = {a0[0], a0[1], a0[2], a0[3], a1[0], a1[1], a1[2], a1[3]};
        oacc[c] = __builtin_amdgcn_mfma_f32_32x32x16_bf16(a, pb[ks], oacc[c], 0, 0, 0);
      }
    }
#pragma unroll
    for (int ks = 0; ks < 2; ++ks)
#pragma unroll
      for (int c = 0; c < 4; ++c) {
        vf[ks][c][0] = tr_read(kbase + va[c][0] + (2 + ks) * 4096);
        vf[ks][c][1] = tr_read(kbase + va[c][1] + (2 + ks) * 4096);
      }
#pragma unroll
    for (int j = 0; j < 16; ++j) { s1[j] = __builtin_amdgcn_exp2f(fmaf(s1[j], cs, -mnew)); ps += s1[j]; }
    l = l * alpha + ps;
#pragma unroll
    for (int sp = 0; sp < 2; ++sp) {
      u32x4 u1 = {pk2(s1[8 * sp + 0], s1[8 * sp + 1]), pk2(s1[8 * sp + 2], s1[8 * sp + 3]),
                  pk2(s1[8 * sp + 4], s1[8 * sp + 5]), pk2(s1[8 * sp + 6], s1[8 * sp + 7])};
      pb[sp] = __builtin_bit_cast(bf16x8, u1);
    }
#pragma unroll
    for (int ks = 0; ks < 2; ++ks) {
#pragma unroll
      for (int c = 0; c < 4; ++c) {
        s16x4 a0 = vf[ks][c][0], a1 = vf[ks][c][1];
        bf16x8 a = {a0[0], a0[1], a0[2], a0[3], a1[0], a1[1], a1[2], a1[3]};
        oacc[c] = __builtin_amdgcn_mfma_f32_32x32x16_bf16(a, pb[ks], oacc[c], 0, 0, 0);
      }
    }
  }
  STAMP(26);
  {
    auto rr = __builtin_amdgcn_permlane32_swap(__float_as_uint(l), __float_as_uint(l), false, false);
    float lt = __uint_as_float(rr[0]) + __uint_as_float(rr[1]);
    float inv = 1.f / lt;
    if (qrow < SQ) {
      char* ob = (char*)OT + ((size_t)((7 * b + (qrow >> 4)) * 128 + 4 * h)) * 1024 + (qrow & 15) * 16 + 8 * hi;
#pragma unroll
      for (int c = 0; c < 4; ++c)
#pragma unroll
        for (int g = 0; g < 4; ++g) {
          u32x2 v = {pk2(oacc[c][4 * g + 0] * inv, oacc[c][4 * g + 1] * inv),
                     pk2(oacc[c][4 * g + 2] * inv, oacc[c][4 * g + 3] * inv)};
          *(u32x2*)(ob + c * 1024 + g * 256) = v;
        }
    }
  }
}

constexpr unsigned G2_LDS = 6 * 24576;
__global__ __launch_bounds__(384, 1) void oproj_kernel(const short* __restrict__ OT, const short* __restrict__ WT,
                                                       short* __restrict__ out) {
  extern __shared__ __attribute__((aligned(16))) char smem[];
  const unsigned lds0 = (unsigned)(uintptr_t)smem;
  const int tid = threadIdx.x;
  const int w = __builtin_amdgcn_readfirstlane(tid >> 6);
  const int lane = tid & 63;
  const int bid = blockIdx.x;
  const int nt = (bid & 7) * 2 + ((bid >> 3) & 1), mt = bid >> 4;
  if (w >= 4) {
    const int L = w - 4;
    const char* Ab = (const char*)OT + ((size_t)((mt * 4 + 2 * L) * 128)) * 1024 + lane * 16;
    const char* Bb = (const char*)WT + ((size_t)((nt * 2 + L) * 128)) * 1024 + lane * 16;
    auto stage = [&](int st, unsigned slot) {
      unsigned base = lds0 + slot * 24576;
#pragma unroll
      for (int mb = 0; mb < 2; ++mb)
#pragma unroll
        for (int k = 0; k < 4; ++k)
          glds16<false>(Ab + (size_t)(mb * 128 + st * 4 + k) * 1024, base + (2 * L + mb) * 4096 + k * 1024);
#pragma unroll
      for (int k = 0; k < 4; ++k) glds16<false>(Bb + (st * 4 + k) * 1024, base + 16384 + L * 4096 + k * 1024);
    };
    stage(0, 0); stage(1, 1); stage(2, 2); stage(3, 3); stage(4, 4);
    unsigned slot = 5;
    for (int st = 0; st < 32; ++st) {
      if (st <= 27) WAITV(48);
      else if (st == 28) WAITV(36);
      else if (st == 29) WAITV(24);
      else if (st == 30) WAITV(12);
      else WAITV(0);
      __builtin_amdgcn_s_barrier();
      if (st + 5 < 32) { stage(st + 5, slot); slot = slot + 1; if (slot >= 6) slot = 0; }
    }
    return;
  }
  f32x4 acc0 = f32x4{}, acc1 = f32x4{};
  unsigned slot = 0;
  for (int st = 0; st < 32; ++st) {
    BARRIER();
    unsigned base = lds0 + slot * 24576 + lane * 16;
    bf16x8 a[4], b0[4], b1[4];
#pragma unroll
    for (int k = 0; k < 4; ++k) {
      a[k] = lds_read16(base + w * 4096 + k * 1024);
      b0[k] = lds_read16(base + 16384 + k * 1024);
      b1[k] = lds_read16(base + 16384 + 4096 + k * 1024);
    }
#pragma unroll
    for (int k = 0; k < 4; ++k) {
      acc0 = __builtin_amdgcn_mfma_f32_16x16x32_bf16(a[k], b0[k], acc0, 0, 0, 0);
      acc1 = __builtin_amdgcn_mfma_f32_16x16x32_bf16(a[k], b1[k], acc1, 0, 0, 0);
    }
    slot = slot + 1; if (slot >= 6) slot = 0;
  }
  const int g = lane >> 4, li = lane & 15;
  const int m0 = mt * 64 + 16 * w + 4 * g, n0 = nt * 32 + li;
#pragma unroll
  for (int j = 0; j < 4; ++j) {
    unsigned v0 = pk2(acc0[j], 0.f), v1 = pk2(acc1[j], 0.f);
    out[(size_t)(m0 + j) * 512 + n0] = (short)(v0 & 0xffff);
    out[(size_t)(m0 + j) * 512 + n0 + 16] = (short)(v1 & 0xffff);
  }
}

extern "C" void kernel_launch(void* const* d_in, const int* in_sizes, int n_in, void* d_out, int out_size,
                              void* d_ws, size_t ws_size, hipStream_t stream) {
  const short* x = (const short*)d_in[0];
  const short* kc = (const short*)d_in[1];
  const short* vc = (const short*)d_in[2];
  const short* wq = (const short*)d_in[3];
  const short* wo = (const short*)d_in[4];
  short* OT = (short*)d_ws;
  short* WT = (short*)((char*)d_ws + (size_t)8 * 1024 * 1024);
  (void)hipFuncSetAttribute((const void*)attn_kernel, hipFuncAttributeMaxDynamicSharedMemorySize, LDS_BYTES);
  (void)hipFuncSetAttribute((const void*)oproj_kernel, hipFuncAttributeMaxDynamicSharedMemorySize, G2_LDS);
  attn_kernel<<<256, 384, LDS_BYTES, stream>>>(x, kc, vc, wq, wo, OT, WT);
  oproj_kernel<<<224, 384, G2_LDS, stream>>>(OT, WT, (short*)d_out);
}
```

```cpp
#include <hip/hip_runtime.h>
#include <hip/hip_bf16.h>
#include <stdint.h>
#include <type_traits>

typedef short bf16x8 __attribute__((ext_vector_type(8)));
typedef short s16x4 __attribute__((ext_vector_type(4)));
typedef float f32x16 __attribute__((ext_vector_type(16)));
typedef float f32x4 __attribute__((ext_vector_type(4)));
typedef float f32x2 __attribute__((ext_vector_type(2)));
typedef unsigned u32x4 __attribute__((ext_vector_type(4)));
typedef unsigned u32x2 __attribute__((ext_vector_type(2)));
typedef __bf16 bf16x2_t __attribute__((ext_vector_type(2)));

constexpr int NB = 8, SQ = 112, DM = 512, NH = 32, SK = 1024, DH = 128;
constexpr int HD = NH * DH;

#define LDS_AS __attribute__((address_space(3)))

__device__ __forceinline__ unsigned pk2(float a, float b) {
  f32x2 v = {a, b};
  bf16x2_t r = __builtin_convertvector(v, bf16x2_t);
  return __builtin_bit_cast(unsigned, r);
}

__device__ __forceinline__ unsigned fsw(unsigned row) { return ((row & 3) << 2) | ((row >> 2) & 3); }

__device__ __forceinline__ s16x4 tr_read(unsigned lds_byte_addr) {
  LDS_AS s16x4* p = (LDS_AS s16x4*)(uintptr_t)lds_byte_addr;
  return __builtin_amdgcn_ds_read_tr16_b64_v4i16(p);
}
__device__ __forceinline__ bf16x8 lds_read16(unsigned lds_byte_addr) {
  LDS_AS bf16x8* p = (LDS_AS bf16x8*)(uintptr_t)lds_byte_addr;
  return *p;
}

template <bool NT>
__device__ __forceinline__ void glds16(const void* gsrc, unsigned lds_dst_uniform) {
  unsigned keep;
  unsigned dst = __builtin_amdgcn_readfirstlane(lds_dst_uniform);
  if constexpr (NT)
    asm volatile("s_mov_b32 %0, m0\n\ts_mov_b32 m0, %2\n\ts_nop 0\n\tglobal_load_lds_dwordx4 %1, off nt\n\ts_mov_b32 m0, %0"
                 : "=&s"(keep) : "v"(gsrc), "s"(dst) : "memory");
  else
    asm volatile("s_mov_b32 %0, m0\n\ts_mov_b32 m0, %2\n\ts_nop 0\n\tglobal_load_lds_dwordx4 %1, off\n\ts_mov_b32 m0, %0"
                 : "=&s"(keep) : "v"(gsrc), "s"(dst) : "memory");
}

#define WAITV(n) asm volatile("s_waitcnt vmcnt(" #n ")" ::: "memory")

constexpr unsigned LDS_BYTES = 163840;
#define BARRIER() do { asm volatile("s_waitcnt lgkmcnt(0)" ::: "memory"); __builtin_amdgcn_s_barrier(); } while (0)

__global__ __launch_bounds__(384, 1) void attn_kernel(const short* __restrict__ x, const short* __restrict__ kc,
                                                      const short* __restrict__ vc, const short* __restrict__ wq,
                                                      const short* __restrict__ wo, short* __restrict__ OT,
                                                      short* __restrict__ WT) {
  extern __shared__ __attribute__((aligned(16))) char smem[];
  const unsigned lds0 = (unsigned)(uintptr_t)smem;
  const int tid = threadIdx.x;
  const int w = __builtin_amdgcn_readfirstlane(tid >> 6);
  const int lane = tid & 63;
  const int r = lane & 31, hi = lane >> 5;
  const int bid = blockIdx.x;
#define STAMP(i)
  STAMP(0);
  const int h = (bid & 7) * 4 + ((bid >> 3) & 3);
  const int b = bid >> 5;

  if (w >= 4) {
    const int L = w - 4;
    const char* Kh = (const char*)(kc + ((size_t)(b * NH + h)) * SK * DH);
    const char* Vh = (const char*)(vc + ((size_t)(b * NH + h)) * SK * DH);
    const char* Wh = (const char*)(wq + h * DH);
    const char* Xb = (const char*)(x + (size_t)b * SQ * DM);
    const char* Wo = (const char*)(wo + (size_t)((bid >> 1) * 32) * 512 + (bid & 1) * 256);
    const unsigned lrow = lane >> 4;
    unsigned so_kv[8], so_w[8], so_x[8], so_o[8];
#pragma unroll
    for (int j = 0; j < 8; ++j) {
      unsigned row = 32 * L + 4 * j + lrow;
      unsigned ch = (lane & 15) ^ ((lrow << 2) | (j & 3));
      so_kv[j] = row * 256 + ch * 16;
      so_w[j] = row * (HD * 2) + ch * 16;
      unsigned xrow = 64 * L + 8 * j + (lane >> 3);
      unsigned xch = (lane & 7) ^ ((xrow >> 1) & 7);
      unsigned xrc = xrow < SQ ? xrow : SQ - 1;
      so_x[j] = xrc * (DM * 2) + xch * 16;
      unsigned orow = 16 * L + 2 * j + (lane >> 5);
      unsigned och = (lane & 31) ^ (((orow & 3) << 2) | ((orow >> 2) & 3));
      so_o[j] = orow * 1024 + och * 16;
    }
    auto dma_w = [&](int t, int half) {
      unsigned dst = lds0 + ((unsigned)t % 3u) * 32768 + half * 16384 + L * 8192;
      if (half) {
        const char* g = Wh + (size_t)t * 64 * (HD * 2);
#pragma unroll
        for (int j = 0; j < 8; ++j) glds16<false>(g + so_w[j], dst + j * 1024);
      } else {
        const char* g = Xb + t * 128;
#pragma unroll
        for (int j = 0; j < 8; ++j) glds16<false>(g + so_x[j], dst + j * 1024);
      }
    };
    auto dma_kv = [&](int i, int half) {
      unsigned pr = (0x02143u >> (4 * ((unsigned)i % 5u))) & 7u;
      unsigned dst = lds0 + pr * 32768 + half * 16384 + L * 8192;
      const char* g = (half ? Vh : Kh) + (size_t)i * 16384;
#pragma unroll
      for (int j = 0; j < 8; ++j) glds16<true>(g + so_kv[j], dst + j * 1024);
    };
    dma_w(0, 0); dma_w(0, 1); dma_kv(0, 0); dma_kv(0, 1); dma_w(1, 0); dma_w(1, 1); dma_kv(1, 0);
    WAITV(40);
    dma_kv(1, 1); dma_w(2, 0); dma_w(2, 1);
    for (int st = 0; st < 25; ++st) {
      if (st == 0) WAITV(56);
      else if (st == 1 || st == 22) WAITV(32);
      else if (st <= 6 || st == 23) WAITV(16);
      else if (st == 7) WAITV(8);
      else if (st == 8 || st == 24) WAITV(0);
      else if (st == 9) WAITV(24);
      else WAITV(48);
      __builtin_amdgcn_s_barrier();
      if (st >= 1 && st <= 5) { dma_w(st + 2, 0); dma_w(st + 2, 1); }
      else if (st == 6) {
#pragma unroll
        for (int j = 0; j < 8; ++j) glds16<false>(Wo + so_o[j], lds0 + 2 * 32768 + L * 8192 + j * 1024);
      } else if (st == 8) {
        dma_kv(2, 0); dma_kv(2, 1);
        const unsigned g = lane >> 4, q4 = (lane & 15) >> 2, p = lane & 3;
        char* dst = (char*)WT + ((size_t)(((bid & 1) * 16 + 8 * L) * 128 + (bid >> 1))) * 1024 + lane * 16;
#pragma unroll
        for (int nt = 0; nt < 8; ++nt) {
          s16x4 t01[2];
#pragma unroll
          for (int t = 0; t < 2; ++t) {
            unsigned row = 8 * g + 4 * t + q4;
            unsigned ch = 2 * (8 * L + nt) + (p >> 1);
            ch ^= ((row & 3) << 2) | ((row >> 2) & 3);
            t01[t] = tr_read(lds0 + 2 * 32768 + row * 512 + ch * 16 + 8 * (p & 1));
          }
          bf16x8 o = {t01[0][0], t01[0][1], t01[0][2], t01[0][3], t01[1][0], t01[1][1], t01[1][2], t01[1][3]};
          *(bf16x8*)(dst + (size_t)nt * 128 * 1024) = o;
        }
        asm volatile("s_waitcnt lgkmcnt(0)" ::: "memory");
      } else if (st == 9) { dma_kv(3, 0); dma_kv(3, 1); dma_kv(4, 0); dma_kv(4, 1); }
      else if (st >= 10 && st <= 20) { dma_kv(st - 5, 0); dma_kv(st - 5, 1); }
    }
    return;
  }

  const unsigned blk = (lane >> 4) & 1, q4 = (lane & 15) >> 2, p = lane & 3;
  const unsigned lo2 = 2 * blk + (p >> 1);
  const int qrow = 32 * w + r;

  f32x16 qacc[4];
#pragma unroll
  for (int c = 0; c < 4; ++c) qacc[c] = f32x16{};
  {
    unsigned wa[4][2];
#pragma unroll
    for (int c = 0; c < 4; ++c)
#pragma unroll
      for (int t = 0; t < 2; ++t)
        wa[c][t] = 16384 + 256 * (8 * hi + 4 * t + q4) + 16 * (4 * (c ^ q4) + (lo2 ^ (2 * hi + t))) + 8 * (p & 1);
    unsigned xa[4];
#pragma unroll
    for (int ks = 0; ks < 4; ++ks) xa[ks] = qrow * 128 + 16 * ((2 * ks + hi) ^ ((qrow >> 1) & 7));
    unsigned xpair = 0;
    for (int t8 = 0; t8 < 8; ++t8) {
      BARRIER();
      STAMP(1 + t8);
      unsigned xbase = lds0 + xpair * 32768;
      xpair += 1; if (xpair >= 3) xpair = 0;
      bf16x8 xf[4];
      s16x4 af[4][4][2];
#pragma unroll
      for (int ks = 0; ks < 4; ++ks) xf[ks] = lds_read16(xbase + xa[ks]);
#pragma unroll
      for (int ks = 0; ks < 4; ++ks)
#pragma unroll
        for (int c = 0; c < 4; ++c) {
          af[ks][c][0] = tr_read(xbase + wa[c][0] + ks * 4096);
          af[ks][c][1] = tr_read(xbase + wa[c][1] + ks * 4096);
        }
#pragma unroll
      for (int ks = 0; ks < 4; ++ks) {
#pragma unroll
        for (int c = 0; c < 4; ++c) {
          s16x4 a0 = af[ks][c][0], a1 = af[ks][c][1];
          bf16x8 a = {a0[0], a0[1], a0[2], a0[3], a1[0], a1[1], a1[2], a1[3]};
          qacc[c] = __builtin_amdgcn_mfma_f32_32x32x16_bf16(a, xf[ks], qacc[c], 0, 0, 0);
        }
      }
    }
  }
  BARRIER();
  STAMP(9);
  bf16x8 qf[8];
  {
    unsigned qs = lds0 + w * 8192;
#pragma unroll
    for (int c = 0; c < 4; ++c)
#pragma unroll
      for (int g = 0; g < 4; ++g) {
        u32x2 v = {pk2(qacc[c][4 * g + 0], qacc[c][4 * g + 1]), pk2(qacc[c][4 * g + 2], qacc[c][4 * g + 3])};
        unsigned ch = 4 * c + g;
        unsigned a = qs + r * 256 + 16 * (ch ^ (r & 15)) + 8 * hi;
        *(LDS_AS u32x2*)(uintptr_t)a = v;
      }
    asm volatile("s_waitcnt lgkmcnt(0)" ::: "memory");
#pragma unroll
    for (int s = 0; s < 8; ++s) {
      unsigned ch = 2 * s + hi;
      qf[s] = lds_read16(qs + r * 256 + 16 * (ch ^ (r & 15)));
    }
  }

  const float cs = 0.08838834764831845f * 1.4426950408889634f;
  float m = -__builtin_inff(), l = 0.f;
  f32x16 oacc[4];
#pragma unroll
  for (int c = 0; c < 4; ++c) oacc[c] = f32x16{};
  unsigned ka[8];
#pragma unroll
  for (int s = 0; s < 8; ++s) ka[s] = 256 * r + 16 * ((2 * s + hi) ^ fsw(r));
  unsigned va[4][2];
#pragma unroll
  for (int c = 0; c < 4; ++c)
#pragma unroll
    for (int t = 0; t < 2; ++t)
      va[c][t] = 16384 + 256 * (8 * t + 4 * hi + q4) + 16 * (4 * (c ^ q4) + (lo2 ^ (2 * t + hi))) + 8 * (p & 1);

  unsigned kph = 0;
  for (int i = 0; i < 16; ++i) {
    BARRIER();
    STAMP(10 + i);
    unsigned kbase = lds0 + ((0x02143u >> (4 * kph)) & 7u) * 32768;
    kph += 1; if (kph >= 5) kph = 0;
    f32x16 s0 = f32x16{}, s1 = f32x16{};
#pragma unroll
    for (int hf = 0; hf < 2; ++hf) {
      bf16x8 kf[4][2];
#pragma unroll
      for (int s = 0; s < 4; ++s) {
        kf[s][0] = lds_read16(kbase + ka[4 * hf + s]);
        kf[s][1] = lds_read16(kbase + ka[4 * hf + s] + 8192);
      }
#pragma unroll
      for (int s = 0; s < 4; ++s) {
        s0 = __builtin_amdgcn_mfma_f32_32x32x16_bf16(kf[s][0], qf[4 * hf + s], s0, 0, 0, 0);
        s1 = __builtin_amdgcn_mfma_f32_32x32x16_bf16(kf[s][1], qf[4 * hf + s], s1, 0, 0, 0);
      }
    }
    s16x4 vf[2][4][2];
#pragma unroll
    for (int ks = 0; ks < 2; ++ks)
#pragma unroll
      for (int c = 0; c < 4; ++c) {
        vf[ks][c][0] = tr_read(kbase + va[c][0] + ks * 4096);
        vf[ks][c][1] = tr_read(kbase + va[c][1] + ks * 4096);
      }
    float mx = s0[0];
#pragma unroll
    for (int j = 1; j < 16; ++j) mx = fmaxf(mx, s0[j]);
#pragma unroll
    for (int j = 0; j < 16; ++j) mx = fmaxf(mx, s1[j]);
    {
      auto rr = __builtin_amdgcn_permlane32_swap(__float_as_uint(mx), __float_as_uint(mx), false, false);
      mx = fmaxf(__uint_as_float(rr[0]), __uint_as_float(rr[1]));
    }
    float mnew = fmaxf(m, mx * cs);
    float alpha = __builtin_amdgcn_exp2f(m - mnew);
    m = mnew;
    if (__builtin_expect(__any(alpha != 1.f), 0)) {
#pragma unroll
      for (int c = 0; c < 4; ++c) oacc[c] *= alpha;
    }
    float ps = 0.f;
#pragma unroll
    for (int j = 0; j < 16; ++j) { s0[j] = __builtin_amdgcn_exp2f(fmaf(s0[j], cs, -mnew)); ps += s0[j]; }
    bf16x8 pb[2];
#pragma unroll
    for (int sp = 0; sp < 2; ++sp) {
      u32x4 u0 = {pk2(s0[8 * sp + 0], s0[8 * sp + 1]), pk2(s0[8 * sp + 2], s0[8 * sp + 3]),
                  pk2(s0[8 * sp + 4], s0[8 * sp + 5]), pk2(s0[8 * sp + 6], s0[8 * sp + 7])};
      pb[sp] = __builtin_bit_cast(bf16x8, u0);
    }
#pragma unroll
    for (int ks = 0; ks < 2; ++ks) {
#pragma unroll
      for (int c = 0; c < 4; ++c) {
        s16x4 a0 = vf[ks][c][0], a1 = vf[ks][c][1];
        bf16x8 a = {a0[0], a0[1], a0[2], a0[3], a1[0], a1[1], a1[2], a1[3]};
        oacc[c] = __builtin_amdgcn_mfma_f32_32x32x16_bf16(a, pb[ks], oacc[c], 0, 0, 0);
      }
    }
#pragma unroll
    for (int ks = 0; ks < 2; ++ks)
#pragma unroll
      for (int c = 0; c < 4; ++c) {
        vf[ks][c][0] = tr_read(kbase + va[c][0] + (2 + ks) * 4096);
        vf[ks][c][1] = tr_read(kbase + va[c][1] + (2 + ks) * 4096);
      }
#pragma unroll
    for (int j = 0; j < 16; ++j) { s1[j] = __builtin_amdgcn_exp2f(fmaf(s1[j], cs, -mnew)); ps += s1[j]; }
    l = l * alpha + ps;
#pragma unroll
    for (int sp = 0; sp < 2; ++sp) {
      u32x4 u1 = {pk2(s1[8 * sp + 0], s1[8 * sp + 1]), pk2(s1[8 * sp + 2], s1[8 * sp + 3]),
                  pk2(s1[8 * sp + 4], s1[8 * sp + 5]), pk2(s1[8 * sp + 6], s1[8 * sp + 7])};
      pb[sp] = __builtin_bit_cast(bf16x8, u1);
    }
#pragma unroll
    for (int ks = 0; ks < 2; ++ks) {
#pragma unroll
      for (int c = 0; c < 4; ++c) {
        s16x4 a0 = vf[ks][c][0], a1 = vf[ks][c][1];
        bf16x8 a = {a0[0], a0[1], a0[2], a0[3], a1[0], a1[1], a1[2], a1[3]};
        oacc[c] = __builtin_amdgcn_mfma_f32_32x32x16_bf16(a, pb[ks], oacc[c], 0, 0, 0);
      }
    }
  }
  STAMP(26);
  {
    auto rr = __builtin_amdgcn_permlane32_swap(__float_as_uint(l), __float_as_uint(l), false, false);
    float lt = __uint_as_float(rr[0]) + __uint_as_float(rr[1]);
    float inv = 1.f / lt;
    if (qrow < SQ) {
      char* ob = (char*)OT + ((size_t)((7 * b + (qrow >> 4)) * 128 + 4 * h)) * 1024 + (qrow & 15) * 16 + 8 * hi;
#pragma unroll
      for (int c = 0; c < 4; ++c)
#pragma unroll
        for (int g = 0; g < 4; ++g) {
          u32x2 v = {pk2(oacc[c][4 * g + 0] * inv, oacc[c][4 * g + 1] * inv),
                     pk2(oacc[c][4 * g + 2] * inv, oacc[c][4 * g + 3] * inv)};
          *(u32x2*)(ob + c * 1024 + g * 256) = v;
        }
    }
  }
}

constexpr unsigned G2_LDS = 6 * 24576;
__global__ __launch_bounds__(384, 1) void oproj_kernel(const short* __restrict__ OT, const short* __restrict__ WT,
                                                       short* __restrict__ out) {
  extern __shared__ __attribute__((aligned(16))) char smem[];
  const unsigned lds0 = (unsigned)(uintptr_t)smem;
  const int tid = threadIdx.x;
  const int w = __builtin_amdgcn_readfirstlane(tid >> 6);
  const int lane = tid & 63;
  const int bid = blockIdx.x;
  const int mt = bid % 14, nt = bid / 14;
  if (w >= 4) {
    const int L = w - 4;
    const char* Ab = (const char*)OT + ((size_t)((mt * 4 + 2 * L) * 128)) * 1024 + lane * 16;
    const char* Bb = (const char*)WT + ((size_t)((nt * 2 + L) * 128)) * 1024 + lane * 16;
    auto stage = [&](int st, unsigned slot) {
      unsigned base = lds0 + slot * 24576;
#pragma unroll
      for (int mb = 0; mb < 2; ++mb)
#pragma unroll
        for (int k = 0; k < 4; ++k)
          glds16<false>(Ab + (size_t)(mb * 128 + st * 4 + k) * 1024, base + (2 * L + mb) * 4096 + k * 1024);
#pragma unroll
      for (int k = 0; k < 4; ++k) glds16<false>(Bb + (st * 4 + k) * 1024, base + 16384 + L * 4096 + k * 1024);
    };
    stage(0, 0); stage(1, 1); stage(2, 2); stage(3, 3); stage(4, 4);
    unsigned slot = 5;
    for (int st = 0; st < 32; ++st) {
      if (st <= 27) WAITV(48);
      else if (st == 28) WAITV(36);
      else if (st == 29) WAITV(24);
      else if (st == 30) WAITV(12);
      else WAITV(0);
      __builtin_amdgcn_s_barrier();
      if (st + 5 < 32) { stage(st + 5, slot); slot = slot + 1; if (slot >= 6) slot = 0; }
    }
    return;
  }
  f32x4 acc0 = f32x4{}, acc1 = f32x4{};
  unsigned slot = 0;
  for (int st = 0; st < 32; ++st) {
    BARRIER();
    unsigned base = lds0 + slot * 24576 + lane * 16;
    bf16x8 a[4], b0[4], b1[4];
#pragma unroll
    for (int k = 0; k < 4; ++k) {
      a[k] = lds_read16(base + w * 4096 + k * 1024);
      b0[k] = lds_read16(base + 16384 + k * 1024);
      b1[k] = lds_read16(base + 16384 + 4096 + k * 1024);
    }
#pragma unroll
    for (int k = 0; k < 4; ++k) {
      acc0 = __builtin_amdgcn_mfma_f32_16x16x32_bf16(a[k], b0[k], acc0, 0, 0, 0);
      acc1 = __builtin_amdgcn_mfma_f32_16x16x32_bf16(a[k], b1[k], acc1, 0, 0, 0);
    }
    slot = slot + 1; if (slot >= 6) slot = 0;
  }
  const int g = lane >> 4, li = lane & 15;
  const int m0 = mt * 64 + 16 * w + 4 * g, n0 = nt * 32 + li;
#pragma unroll
  for (int j = 0; j < 4; ++j) {
    unsigned v0 = pk2(acc0[j], 0.f), v1 = pk2(acc1[j], 0.f);
    out[(size_t)(m0 + j) * 512 + n0] = (short)(v0 & 0xffff);
    out[(size_t)(m0 + j) * 512 + n0 + 16] = (short)(v1 & 0xffff);
  }
}

extern "C" void kernel_launch(void* const* d_in, const int* in_sizes, int n_in, void* d_out, int out_size,
                              void* d_ws, size_t ws_size, hipStream_t stream) {
  const short* x = (const short*)d_in[0];
  const short* kc = (const short*)d_in[1];
  const short* vc = (const short*)d_in[2];
  const short* wq = (const short*)d_in[3];
  const short* wo = (const short*)d_in[4];
  short* OT = (short*)d_ws;
  short* WT = (short*)((char*)d_ws + (size_t)8 * 1024 * 1024);
  (void)hipFuncSetAttribute((const void*)attn_kernel, hipFuncAttributeMaxDynamicSharedMemorySize, LDS_BYTES);
  (void)hipFuncSetAttribute((const void*)oproj_kernel, hipFuncAttributeMaxDynamicSharedMemorySize, G2_LDS);
  attn_kernel<<<256, 384, LDS_BYTES, stream>>>(x, kc, vc, wq, wo, OT, WT);
  oproj_kernel<<<224, 384, G2_LDS, stream>>>(OT, WT, (short*)d_out);
}
```

```cpp
#include <hip/hip_runtime.h>
#include <hip/hip_bf16.h>
#include <stdint.h>
#include <type_traits>

typedef short bf16x8 __attribute__((ext_vector_type(8)));
typedef short s16x4 __attribute__((ext_vector_type(4)));
typedef float f32x16 __attribute__((ext_vector_type(16)));
typedef float f32x4 __attribute__((ext_vector_type(4)));
typedef float f32x2 __attribute__((ext_vector_type(2)));
typedef unsigned u32x4 __attribute__((ext_vector_type(4)));
typedef unsigned u32x2 __attribute__((ext_vector_type(2)));
typedef __bf16 bf16x2_t __attribute__((ext_vector_type(2)));

constexpr int NB = 8, SQ = 112, DM = 512, NH = 32, SK = 1024, DH = 128;
constexpr int HD = NH * DH;

#define LDS_AS __attribute__((address_space(3)))

__device__ __forceinline__ unsigned pk2(float a, float b) {
  f32x2 v = {a, b};
  bf16x2_t r = __builtin_convertvector(v, bf16x2_t);
  return __builtin_bit_cast(unsigned, r);
}

__device__ __forceinline__ unsigned fsw(unsigned row) { return ((row & 3) << 2) | ((row >> 2) & 3); }

__device__ __forceinline__ s16x4 tr_read(unsigned lds_byte_addr) {
  LDS_AS s16x4* p = (LDS_AS s16x4*)(uintptr_t)lds_byte_addr;
  return __builtin_amdgcn_ds_read_tr16_b64_v4i16(p);
}
__device__ __forceinline__ bf16x8 lds_read16(unsigned lds_byte_addr) {
  LDS_AS bf16x8* p = (LDS_AS bf16x8*)(uintptr_t)lds_byte_addr;
  return *p;
}

template <bool NT>
__device__ __forceinline__ void glds16(const void* gsrc, unsigned lds_dst_uniform) {
  unsigned keep;
  unsigned dst = __builtin_amdgcn_readfirstlane(lds_dst_uniform);
  if constexpr (NT)
    asm volatile("s_mov_b32 %0, m0\n\ts_mov_b32 m0, %2\n\ts_nop 0\n\tglobal_load_lds_dwordx4 %1, off nt\n\ts_mov_b32 m0, %0"
                 : "=&s"(keep) : "v"(gsrc), "s"(dst) : "memory");
  else
    asm volatile("s_mov_b32 %0, m0\n\ts_mov_b32 m0, %2\n\ts_nop 0\n\tglobal_load_lds_dwordx4 %1, off\n\ts_mov_b32 m0, %0"
                 : "=&s"(keep) : "v"(gsrc), "s"(dst) : "memory");
}

#define WAITV(n) asm volatile("s_waitcnt vmcnt(" #n ")" ::: "memory")

constexpr unsigned LDS_BYTES = 163840;
#define BARRIER() do { asm volatile("s_waitcnt lgkmcnt(0)" ::: "memory"); __builtin_amdgcn_s_barrier(); } while (0)

__global__ __launch_bounds__(384, 1) void attn_kernel(const short* __restrict__ x, const short* __restrict__ kc,
                                                      const short* __restrict__ vc, const short* __restrict__ wq,
                                                      const short* __restrict__ wo, short* __restrict__ OT,
                                                      short* __restrict__ WT) {
  extern __shared__ __attribute__((aligned(16))) char smem[];
  const unsigned lds0 = (unsigned)(uintptr_t)smem;
  const int tid = threadIdx.x;
  const int w = __builtin_amdgcn_readfirstlane(tid >> 6);
  const int lane = tid & 63;
  const int r = lane & 31, hi = lane >> 5;
  const int bid = blockIdx.x;
  const int h = (bid & 7) * 4 + ((bid >> 3) & 3);
  const int b = bid >> 5;

  if (w >= 4) {
    const int L = w - 4;
    const char* Kh = (const char*)(kc + ((size_t)(b * NH + h)) * SK * DH);
    const char* Vh = (const char*)(vc + ((size_t)(b * NH + h)) * SK * DH);
    const char* Wh = (const char*)(wq + h * DH);
    const char* Xb = (const char*)(x + (size_t)b * SQ * DM);
    const char* Wo = (const char*)(wo + (size_t)((bid >> 1) * 32) * 512 + (bid & 1) * 256);
    const unsigned lrow = lane >> 4;
    unsigned so_kv[8], so_w[8], so_x[8], so_o[8];
#pragma unroll
    for (int j = 0; j < 8; ++j) {
      unsigned row = 32 * L + 4 * j + lrow;
      unsigned ch = (lane & 15) ^ ((lrow << 2) | (j & 3));
      so_kv[j] = row * 256 + ch * 16;
      so_w[j] = row * (HD * 2) + ch * 16;
      unsigned xrow = 64 * L + 8 * j + (lane >> 3);
      unsigned xch = (lane & 7) ^ ((xrow >> 1) & 7);
      unsigned xrc = xrow < SQ ? xrow : SQ - 1;
      so_x[j] = xrc * (DM * 2) + xch * 16;
      unsigned orow = 16 * L + 2 * j + (lane >> 5);
      unsigned och = (lane & 31) ^ (((orow & 3) << 2) | ((orow >> 2) & 3));
      so_o[j] = orow * 1024 + och * 16;
    }
    auto dma_item = [&](int n) {
      unsigned slot = (unsigned)n % 10u;
      unsigned dst = lds0 + slot * 16384 + L * 8192;
      if (n < 16) {
        int t = n >> 1;
        if (n & 1) {
          const char* g = Wh + (size_t)t * 64 * (HD * 2);
#pragma unroll
          for (int j = 0; j < 8; ++j) glds16<false>(g + so_w[j], dst + j * 1024);
        } else {
          const char* g = Xb + t * 128;
#pragma unroll
          for (int j = 0; j < 8; ++j) glds16<false>(g + so_x[j], dst + j * 1024);
        }
      } else if (n == 16) {
#pragma unroll
        for (int j = 0; j < 8; ++j) glds16<false>(Wo + so_o[j], dst + j * 1024);
      } else if (n >= 18) {
        int t = (n - 18) >> 1;
        const char* g = ((n & 1) ? Vh : Kh) + (size_t)t * 16384;
#pragma unroll
        for (int j = 0; j < 8; ++j) glds16<true>(g + so_kv[j], dst + j * 1024);
      }
    };
    for (int n = 0; n < 7; ++n) dma_item(n);
    WAITV(32);
    for (int n = 7; n < 10; ++n) dma_item(n);
    for (int st = 0; st < 25; ++st) {
      if (st == 0) WAITV(56);
      else if (st >= 5 && st <= 7) WAITV(40);
      else if (st == 9 || st == 22) WAITV(32);
      else if (st == 23) WAITV(16);
      else if (st == 24) WAITV(0);
      else WAITV(48);
      __builtin_amdgcn_s_barrier();
      if (st == 9) { dma_item(24); dma_item(25); dma_item(26); dma_item(27); }
      else if (st >= 1 && st <= 20 && st != 8) { dma_item(2 * st + 8); dma_item(2 * st + 9); }
    }
    return;
  }

  const unsigned blk = (lane >> 4) & 1, q4 = (lane & 15) >> 2, p = lane & 3;
  const unsigned lo2 = 2 * blk + (p >> 1);
  const int qrow = 32 * w + r;

  f32x16 qacc[4];
#pragma unroll
  for (int c = 0; c < 4; ++c) qacc[c] = f32x16{};
  {
    unsigned wa[4][2];
#pragma unroll
    for (int c = 0; c < 4; ++c)
#pragma unroll
      for (int t = 0; t < 2; ++t)
        wa[c][t] = 16384 + 256 * (8 * hi + 4 * t + q4) + 16 * (4 * (c ^ q4) + (lo2 ^ (2 * hi + t))) + 8 * (p & 1);
    unsigned xa[4];
#pragma unroll
    for (int ks = 0; ks < 4; ++ks) xa[ks] = qrow * 128 + 16 * ((2 * ks + hi) ^ ((qrow >> 1) & 7));
    unsigned xslot = 0;
    for (int t8 = 0; t8 < 8; ++t8) {
      BARRIER();
      unsigned xbase = lds0 + xslot * 16384;
      xslot += 2; if (xslot >= 10) xslot -= 10;
      bf16x8 xf[4];
      s16x4 af[4][4][2];
#pragma unroll
      for (int ks = 0; ks < 4; ++ks) xf[ks] = lds_read16(xbase + xa[ks]);
#pragma unroll
      for (int ks = 0; ks < 4; ++ks)
#pragma unroll
        for (int c = 0; c < 4; ++c) {
          af[ks][c][0] = tr_read(xbase + wa[c][0] + ks * 4096);
          af[ks][c][1] = tr_read(xbase + wa[c][1] + ks * 4096);
        }
#pragma unroll
      for (int ks = 0; ks < 4; ++ks) {
#pragma unroll
        for (int c = 0; c < 4; ++c) {
          s16x4 a0 = af[ks][c][0], a1 = af[ks][c][1];
          bf16x8 a = {a0[0], a0[1], a0[2], a0[3], a1[0], a1[1], a1[2], a1[3]};
          qacc[c] = __builtin_amdgcn_mfma_f32_32x32x16_bf16(a, xf[ks], qacc[c], 0, 0, 0);
        }
      }
    }
  }
  BARRIER();
  bf16x8 qf[8];
  {
    unsigned qs = lds0 + 4 * 16384 + w * 8192;
#pragma unroll
    for (int c = 0; c < 4; ++c)
#pragma unroll
      for (int g = 0; g < 4; ++g) {
        u32x2 v = {pk2(qacc[c][4 * g + 0], qacc[c][4 * g + 1]), pk2(qacc[c][4 * g + 2], qacc[c][4 * g + 3])};
        unsigned ch = 4 * c + g;
        unsigned a = qs + r * 256 + 16 * (ch ^ (r & 15)) + 8 * hi;
        *(LDS_AS u32x2*)(uintptr_t)a = v;
      }
    {
      const unsigned g = lane >> 4;
      char* dst = (char*)WT + ((size_t)(((bid & 1) * 16 + 4 * w) * 128 + (bid >> 1))) * 1024 + lane * 16;
#pragma unroll
      for (int nt = 0; nt < 4; ++nt) {
        s16x4 t01[2];
#pragma unroll
        for (int t = 0; t < 2; ++t) {
          unsigned row = 8 * g + 4 * t + q4;
          unsigned ch = 2 * (4 * w + nt) + (p >> 1);
          ch ^= ((row & 3) << 2) | ((row >> 2) & 3);
          t01[t] = tr_read(lds0 + 6 * 16384 + row * 512 + ch * 16 + 8 * (p & 1));
        }
        bf16x8 o = {t01[0][0], t01[0][1], t01[0][2], t01[0][3], t01[1][0], t01[1][1], t01[1][2], t01[1][3]};
        *(bf16x8*)(dst + (size_t)nt * 128 * 1024) = o;
      }
    }
    asm volatile("s_waitcnt lgkmcnt(0)" ::: "memory");
#pragma unroll
    for (int s = 0; s < 8; ++s) {
      unsigned ch = 2 * s + hi;
      qf[s] = lds_read16(qs + r * 256 + 16 * (ch ^ (r & 15)));
    }
  }

  const float cs = 0.08838834764831845f * 1.4426950408889634f;
  float m = -__builtin_inff(), l = 0.f;
  f32x16 oacc[4];
#pragma unroll
  for (int c = 0; c < 4; ++c) oacc[c] = f32x16{};
  unsigned ka[8];
#pragma unroll
  for (int s = 0; s < 8; ++s) ka[s] = 256 * r + 16 * ((2 * s + hi) ^ fsw(r));
  unsigned va[4][2];
#pragma unroll
  for (int c = 0; c < 4; ++c)
#pragma unroll
    for (int t = 0; t < 2; ++t)
      va[c][t] = 16384 + 256 * (8 * t + 4 * hi + q4) + 16 * (4 * (c ^ q4) + (lo2 ^ (2 * t + hi))) + 8 * (p & 1);

  unsigned kslot = 8;
  for (int i = 0; i < 16; ++i) {
    BARRIER();
    unsigned kbase = lds0 + kslot * 16384;
    kslot += 2; if (kslot >= 10) kslot -= 10;
    f32x16 s0 = f32x16{}, s1 = f32x16{};
#pragma unroll
    for (int hf = 0; hf < 2; ++hf) {
      bf16x8 kf[4][2];
#pragma unroll
      for (int s = 0; s < 4; ++s) {
        kf[s][0] = lds_read16(kbase + ka[4 * hf + s]);
        kf[s][1] = lds_read16(kbase + ka[4 * hf + s] + 8192);
      }
#pragma unroll
      for (int s = 0; s < 4; ++s) {
        s0 = __builtin_amdgcn_mfma_f32_32x32x16_bf16(kf[s][0], qf[4 * hf + s], s0, 0, 0, 0);
        s1 = __builtin_amdgcn_mfma_f32_32x32x16_bf16(kf[s][1], qf[4 * hf + s], s1, 0, 0, 0);
      }
    }
    s16x4 vf[2][4][2];
#pragma unroll
    for (int ks = 0; ks < 2; ++ks)
#pragma unroll
      for (int c = 0; c < 4; ++c) {
        vf[ks][c][0] = tr_read(kbase + va[c][0] + ks * 4096);
        vf[ks][c][1] = tr_read(kbase + va[c][1] + ks * 4096);
      }
    float mx = s0[0];
#pragma unroll
    for (int j = 1; j < 16; ++j) mx = fmaxf(mx, s0[j]);
#pragma unroll
    for (int j = 0; j < 16; ++j) mx = fmaxf(mx, s1[j]);
    {
      auto rr = __builtin_amdgcn_permlane32_swap(__float_as_uint(mx), __float_as_uint(mx), false, false);
      mx = fmaxf(__uint_as_float(rr[0]), __uint_as_float(rr[1]));
    }
    float mnew = fmaxf(m, mx * cs);
    float alpha = __builtin_amdgcn_exp2f(m - mnew);
    m = mnew;
    if (__builtin_expect(__any(alpha != 1.f), 0)) {
#pragma unroll
      for (int c = 0; c < 4; ++c) oacc[c] *= alpha;
    }
    float ps = 0.f;
#pragma unroll
    for (int j = 0; j < 16; ++j) { s0[j] = __builtin_amdgcn_exp2f(fmaf(s0[j], cs, -mnew)); ps += s0[j]; }
    bf16x8 pb[2];
#pragma unroll
    for (int sp = 0; sp < 2; ++sp) {
      u32x4 u0 = {pk2(s0[8 * sp + 0], s0[8 * sp + 1]), pk2(s0[8 * sp + 2], s0[8 * sp + 3]),
                  pk2(s0[8 * sp + 4], s0[8 * sp + 5]), pk2(s0[8 * sp + 6], s0[8 * sp + 7])};
      pb[sp] = __builtin_bit_cast(bf16x8, u0);
    }
#pragma unroll
    for (int ks = 0; ks < 2; ++ks) {
#pragma unroll
      for (int c = 0; c < 4; ++c) {
        s16x4 a0 = vf[ks][c][0], a1 = vf[ks][c][1];
        bf16x8 a = {a0[0], a0[1], a0[2], a0[3], a1[0], a1[1], a1[2], a1[3]};
        oacc[c] = __builtin_amdgcn_mfma_f32_32x32x16_bf16(a, pb[ks], oacc[c], 0, 0, 0);
      }
    }
#pragma unroll
    for (int ks = 0; ks < 2; ++ks)
#pragma unroll
      for (int c = 0; c < 4; ++c) {
        vf[ks][c][0] = tr_read(kbase + va[c][0] + (2 + ks) * 4096);
        vf[ks][c][1] = tr_read(kbase + va[c][1] + (2 + ks) * 4096);
      }
#pragma unroll
    for (int j = 0; j < 16; ++j) { s1[j] = __builtin_amdgcn_exp2f(fmaf(s1[j], cs, -mnew)); ps += s1[j]; }
    l = l * alpha + ps;
#pragma unroll
    for (int sp = 0; sp < 2; ++sp) {
      u32x4 u1 = {pk2(s1[8 * sp + 0], s1[8 * sp + 1]), pk2(s1[8 * sp + 2], s1[8 * sp + 3]),
                  pk2(s1[8 * sp + 4], s1[8 * sp + 5]), pk2(s1[8 * sp + 6], s1[8 * sp + 7])};
      pb[sp] = __builtin_bit_cast(bf16x8, u1);
    }
#pragma unroll
    for (int ks = 0; ks < 2; ++ks) {
#pragma unroll
      for (int c = 0; c < 4; ++c) {
        s16x4 a0 = vf[ks][c][0], a1 = vf[ks][c][1];
        bf16x8 a = {a0[0], a0[1], a0[2], a0[3], a1[0], a1[1], a1[2], a1[3]};
        oacc[c] = __builtin_amdgcn_mfma_f32_32x32x16_bf16(a, pb[ks], oacc[c], 0, 0, 0);
      }
    }
  }
  {
    auto rr = __builtin_amdgcn_permlane32_swap(__float_as_uint(l), __float_as_uint(l), false, false);
    float lt = __uint_as_float(rr[0]) + __uint_as_float(rr[1]);
    float inv = 1.f / lt;
    if (qrow < SQ) {
      char* ob = (char*)OT + ((size_t)((7 * b + (qrow >> 4)) * 128 + 4 * h)) * 1024 + (qrow & 15) * 16 + 8 * hi;
#pragma unroll
      for (int c = 0; c < 4; ++c)
#pragma unroll
        for (int g = 0; g < 4; ++g) {
          u32x2 v = {pk2(oacc[c][4 * g + 0] * inv, oacc[c][4 * g + 1] * inv),
                     pk2(oacc[c][4 * g + 2] * inv, oacc[c][4 * g + 3] * inv)};
          *(u32x2*)(ob + c * 1024 + g * 256) = v;
        }
    }
  }
}

constexpr unsigned G2_LDS = 6 * 24576;
__global__ __launch_bounds__(384, 1) void oproj_kernel(const short* __restrict__ OT, const short* __restrict__ WT,
                                                       short* __restrict__ out) {
  extern __shared__ __attribute__((aligned(16))) char smem[];
  const unsigned lds0 = (unsigned)(uintptr_t)smem;
  const int tid = threadIdx.x;
  const int w = __builtin_amdgcn_readfirstlane(tid >> 6);
  const int lane = tid & 63;
  const int bid = blockIdx.x;
  const int mt = bid % 14, nt = bid / 14;
  if (w >= 4) {
    const int L = w - 4;
    const char* Ab = (const char*)OT + ((size_t)((mt * 4 + 2 * L) * 128)) * 1024 + lane * 16;
    const char* Bb = (const char*)WT + ((size_t)((nt * 2 + L) * 128)) * 1024 + lane * 16;
    auto stage = [&](int st, unsigned slot) {
      unsigned base = lds0 + slot * 24576;
#pragma unroll
      for (int mb = 0; mb < 2; ++mb)
#pragma unroll
        for (int k = 0; k < 4; ++k)
          glds16<false>(Ab + (size_t)(mb * 128 + st * 4 + k) * 1024, base + (2 * L + mb) * 4096 + k * 1024);
#pragma unroll
      for (int k = 0; k < 4; ++k) glds16<false>(Bb + (st * 4 + k) * 1024, base + 16384 + L * 4096 + k * 1024);
    };
    stage(0, 0); stage(1, 1); stage(2, 2); stage(3, 3); stage(4, 4);
    unsigned slot = 5;
    for (int st = 0; st < 32; ++st) {
      if (st <= 27) WAITV(48);
      else if (st == 28) WAITV(36);
      else if (st == 29) WAITV(24);
      else if (st == 30) WAITV(12);
      else WAITV(0);
      __builtin_amdgcn_s_barrier();
      if (st + 5 < 32) { stage(st + 5, slot); slot = slot + 1; if (slot >= 6) slot = 0; }
    }
    return;
  }
  f32x4 acc0 = f32x4{}, acc1 = f32x4{};
  unsigned slot = 0;
  for (int st = 0; st < 32; ++st) {
    BARRIER();
    unsigned base = lds0 + slot * 24576 + lane * 16;
    bf16x8 a[4], b0[4], b1[4];
#pragma unroll
    for (int k = 0; k < 4; ++k) {
      a[k] = lds_read16(base + w * 4096 + k * 1024);
      b0[k] = lds_read16(base + 16384 + k * 1024);
      b1[k] = lds_read16(base + 16384 + 4096 + k * 1024);
    }
#pragma unroll
    for (int k = 0; k < 4; ++k) {
      acc0 = __builtin_amdgcn_mfma_f32_16x16x32_bf16(a[k], b0[k], acc0, 0, 0, 0);
      acc1 = __builtin_amdgcn_mfma_f32_16x16x32_bf16(a[k], b1[k], acc1, 0, 0, 0);
    }
    slot = slot + 1; if (slot >= 6) slot = 0;
  }
  const int g = lane >> 4, li = lane & 15;
  const int m0 = mt * 64 + 16 * w + 4 * g, n0 = nt * 32 + li;
#pragma unroll
  for (int j = 0; j < 4; ++j) {
    unsigned v0 = pk2(acc0[j], 0.f), v1 = pk2(acc1[j], 0.f);
    out[(size_t)(m0 + j) * 512 + n0] = (short)(v0 & 0xffff);
    out[(size_t)(m0 + j) * 512 + n0 + 16] = (short)(v1 & 0xffff);
  }
}

extern "C" void kernel_launch(void* const* d_in, const int* in_sizes, int n_in, void* d_out, int out_size,
                              void* d_ws, size_t ws_size, hipStream_t stream) {
  const short* x = (const short*)d_in[0];
  const short* kc = (const short*)d_in[1];
  const short* vc = (const short*)d_in[2];
  const short* wq = (const short*)d_in[3];
  const short* wo = (const short*)d_in[4];
  short* OT = (short*)d_ws;
  short* WT = (short*)((char*)d_ws + (size_t)8 * 1024 * 1024);
  (void)hipFuncSetAttribute((const void*)attn_kernel, hipFuncAttributeMaxDynamicSharedMemorySize, LDS_BYTES);
  (void)hipFuncSetAttribute((const void*)oproj_kernel, hipFuncAttributeMaxDynamicSharedMemorySize, G2_LDS);
  attn_kernel<<<256, 384, LDS_BYTES, stream>>>(x, kc, vc, wq, wo, OT, WT);
  oproj_kernel<<<224, 384, G2_LDS, stream>>>(OT, WT, (short*)d_out);
}
```

```cpp
#include <hip/hip_runtime.h>
#include <hip/hip_bf16.h>
#include <stdint.h>
#include <type_traits>

typedef short bf16x8 __attribute__((ext_vector_type(8)));
typedef short s16x4 __attribute__((ext_vector_type(4)));
typedef float f32x16 __attribute__((ext_vector_type(16)));
typedef float f32x4 __attribute__((ext_vector_type(4)));
typedef float f32x2 __attribute__((ext_vector_type(2)));
typedef unsigned u32x4 __attribute__((ext_vector_type(4)));
typedef unsigned u32x2 __attribute__((ext_vector_type(2)));
typedef __bf16 bf16x2_t __attribute__((ext_vector_type(2)));

constexpr int NB = 8, SQ = 112, DM = 512, NH = 32, SK = 1024, DH = 128;
constexpr int HD = NH * DH;

#define LDS_AS __attribute__((address_space(3)))

__device__ __forceinline__ unsigned pk2(float a, float b) {
  f32x2 v = {a, b};
  bf16x2_t r = __builtin_convertvector(v, bf16x2_t);
  return __builtin_bit_cast(unsigned, r);
}

__device__ __forceinline__ unsigned fsw(unsigned row) { return ((row & 3) << 2) | ((row >> 2) & 3); }

__device__ __forceinline__ s16x4 tr_read(unsigned lds_byte_addr) {
  LDS_AS s16x4* p = (LDS_AS s16x4*)(uintptr_t)lds_byte_addr;
  return __builtin_amdgcn_ds_read_tr16_b64_v4i16(p);
}
__device__ __forceinline__ bf16x8 lds_read16(unsigned lds_byte_addr) {
  LDS_AS bf16x8* p = (LDS_AS bf16x8*)(uintptr_t)lds_byte_addr;
  return *p;
}

template <bool NT>
__device__ __forceinline__ void glds16(const void* gsrc, unsigned lds_dst_uniform) {
  unsigned keep;
  unsigned dst = __builtin_amdgcn_readfirstlane(lds_dst_uniform);
  if constexpr (NT)
    asm volatile("s_mov_b32 %0, m0\n\ts_mov_b32 m0, %2\n\ts_nop 0\n\tglobal_load_lds_dwordx4 %1, off nt\n\ts_mov_b32 m0, %0"
                 : "=&s"(keep) : "v"(gsrc), "s"(dst) : "memory");
  else
    asm volatile("s_mov_b32 %0, m0\n\ts_mov_b32 m0, %2\n\ts_nop 0\n\tglobal_load_lds_dwordx4 %1, off\n\ts_mov_b32 m0, %0"
                 : "=&s"(keep) : "v"(gsrc), "s"(dst) : "memory");
}

#define WAITV(n) asm volatile("s_waitcnt vmcnt(" #n ")" ::: "memory")

constexpr unsigned LDS_BYTES = 163840;
#define BARRIER() do { asm volatile("s_waitcnt lgkmcnt(0)" ::: "memory"); __builtin_amdgcn_s_barrier(); } while (0)

__global__ __launch_bounds__(384, 1) void attn_kernel(const short* __restrict__ x, const short* __restrict__ kc,
                                                      const short* __restrict__ vc, const short* __restrict__ wq,
                                                      const short* __restrict__ wo, short* __restrict__ OT,
                                                      short* __restrict__ WT) {
  extern __shared__ __attribute__((aligned(16))) char smem[];
  const unsigned lds0 = (unsigned)(uintptr_t)smem;
  const int tid = threadIdx.x;
  const int w = __builtin_amdgcn_readfirstlane(tid >> 6);
  const int lane = tid & 63;
  const int r = lane & 31, hi = lane >> 5;
  const int bid = blockIdx.x;
  const int h = (bid & 7) * 4 + ((bid >> 3) & 3);
  const int b = bid >> 5;

  if (w >= 4) {
    const int L = w - 4;
    const char* Kh = (const char*)(kc + ((size_t)(b * NH + h)) * SK * DH);
    const char* Vh = (const char*)(vc + ((size_t)(b * NH + h)) * SK * DH);
    const char* Wh = (const char*)(wq + h * DH);
    const char* Xb = (const char*)(x + (size_t)b * SQ * DM);
    const char* Wo = (const char*)(wo + (size_t)((bid >> 1) * 32) * 512 + (bid & 1) * 256);
    const unsigned lrow = lane >> 4;
    unsigned so_kv[8], so_w[8], so_x[8], so_o[8];
#pragma unroll
    for (int j = 0; j < 8; ++j) {
      unsigned row = 32 * L + 4 * j + lrow;
      unsigned ch = (lane & 15) ^ ((lrow << 2) | (j & 3));
      so_kv[j] = row * 256 + ch * 16;
      so_w[j] = row * (HD * 2) + ch * 16;
      unsigned xrow = 64 * L + 8 * j + (lane >> 3);
      unsigned xch = (lane & 7) ^ ((xrow >> 1) & 7);
      unsigned xrc = xrow < SQ ? xrow : SQ - 1;
      so_x[j] = xrc * (DM * 2) + xch * 16;
      unsigned orow = 16 * L + 2 * j + (lane >> 5);
      unsigned och = (lane & 31) ^ (((orow & 3) << 2) | ((orow >> 2) & 3));
      so_o[j] = orow * 1024 + och * 16;
    }
    auto dma_item = [&](int n) {
      unsigned slot = (unsigned)n % 10u;
      unsigned dst = lds0 + slot * 16384 + L * 8192;
      if (n < 16) {
        int t = n >> 1;
        if (n & 1) {
          const char* g = Wh + (size_t)t * 64 * (HD * 2);
#pragma unroll
          for (int j = 0; j < 8; ++j) glds16<false>(g + so_w[j], dst + j * 1024);
        } else {
          const char* g = Xb + t * 128;
#pragma unroll
          for (int j = 0; j < 8; ++j) glds16<false>(g + so_x[j], dst + j * 1024);
        }
      } else if (n == 16) {
#pragma unroll
        for (int j = 0; j < 8; ++j) glds16<false>(Wo + so_o[j], dst + j * 1024);
      } else if (n >= 18) {
        int t = (n - 18) >> 1;
        const char* g = ((n & 1) ? Vh : Kh) + (size_t)t * 16384;
#pragma unroll
        for (int j = 0; j < 8; ++j) glds16<true>(g + so_kv[j], dst + j * 1024);
      }
    };
    for (int n = 0; n < 7; ++n) dma_item(n);
    WAITV(32);
    for (int n = 7; n < 10; ++n) dma_item(n);
    for (int st = 0; st < 25; ++st) {
      if (st == 0) WAITV(56);
      else if (st >= 5 && st <= 7) WAITV(40);
      else if (st == 9 || st == 22) WAITV(32);
      else if (st == 23) WAITV(16);
      else if (st == 24) WAITV(0);
      else WAITV(48);
      __builtin_amdgcn_s_barrier();
      if (st == 9) { dma_item(24); dma_item(25); dma_item(26); dma_item(27); }
      else if (st >= 1 && st <= 20 && st != 8) { dma_item(2 * st + 8); dma_item(2 * st + 9); }
    }
    return;
  }

  const unsigned blk = (lane >> 4) & 1, q4 = (lane & 15) >> 2, p = lane & 3;
  const unsigned lo2 = 2 * blk + (p >> 1);
  const int qrow = 32 * w + r;

  f32x16 qacc[4];
#pragma unroll
  for (int c = 0; c < 4; ++c) qacc[c] = f32x16{};
  {
    unsigned wa[4][2];
#pragma unroll
    for (int c = 0; c < 4; ++c)
#pragma unroll
      for (int t = 0; t < 2; ++t)
        wa[c][t] = 16384 + 256 * (8 * hi + 4 * t + q4) + 16 * (4 * (c ^ q4) + (lo2 ^ (2 * hi + t))) + 8 * (p & 1);
    unsigned xa[4];
#pragma unroll
    for (int ks = 0; ks < 4; ++ks) xa[ks] = qrow * 128 + 16 * ((2 * ks + hi) ^ ((qrow >> 1) & 7));
    unsigned xslot = 0;
    for (int t8 = 0; t8 < 8; ++t8) {
      BARRIER();
      unsigned xbase = lds0 + xslot * 16384;
      xslot += 2; if (xslot >= 10) xslot -= 10;
      bf16x8 xf[4];
      s16x4 af[4][4][2];
#pragma unroll
      for (int ks = 0; ks < 4; ++ks) xf[ks] = lds_read16(xbase + xa[ks]);
#pragma unroll
      for (int ks = 0; ks < 4; ++ks)
#pragma unroll
        for (int c = 0; c < 4; ++c) {
          af[ks][c][0] = tr_read(xbase + wa[c][0] + ks * 4096);
          af[ks][c][1] = tr_read(xbase + wa[c][1] + ks * 4096);
        }
#pragma unroll
      for (int ks = 0; ks < 4; ++ks) {
#pragma unroll
        for (int c = 0; c < 4; ++c) {
          s16x4 a0 = af[ks][c][0], a1 = af[ks][c][1];
          bf16x8 a = {a0[0], a0[1], a0[2], a0[3], a1[0], a1[1], a1[2], a1[3]};
          qacc[c] = __builtin_amdgcn_mfma_f32_32x32x16_bf16(a, xf[ks], qacc[c], 0, 0, 0);
        }
      }
    }
  }
  BARRIER();
  bf16x8 qf[8];
  {
    unsigned qs = lds0 + 4 * 16384 + w * 8192;
#pragma unroll
    for (int c = 0; c < 4; ++c)
#pragma unroll
      for (int g = 0; g < 4; ++g) {
        u32x2 v = {pk2(qacc[c][4 * g + 0], qacc[c][4 * g + 1]), pk2(qacc[c][4 * g + 2], qacc[c][4 * g + 3])};
        unsigned ch = 4 * c + g;
        unsigned a = qs + r * 256 + 16 * (ch ^ (r & 15)) + 8 * hi;
        *(LDS_AS u32x2*)(uintptr_t)a = v;
      }
    {
      const unsigned g = lane >> 4;
      char* dst = (char*)WT + ((size_t)(((bid & 1) * 16 + 4 * w) * 128 + (bid >> 1))) * 1024 + lane * 16;
#pragma unroll
      for (int nt = 0; nt < 4; ++nt) {
        s16x4 t01[2];
#pragma unroll
        for (int t = 0; t < 2; ++t) {
          unsigned row = 8 * g + 4 * t + q4;
          unsigned ch = 2 * (4 * w + nt) + (p >> 1);
          ch ^= ((row & 3) << 2) | ((row >> 2) & 3);
          t01[t] = tr_read(lds0 + 6 * 16384 + row * 512 + ch * 16 + 8 * (p & 1));
        }
        bf16x8 o = {t01[0][0], t01[0][1], t01[0][2], t01[0][3], t01[1][0], t01[1][1], t01[1][2], t01[1][3]};
        *(bf16x8*)(dst + (size_t)nt * 128 * 1024) = o;
      }
    }
    asm volatile("s_waitcnt lgkmcnt(0)" ::: "memory");
#pragma unroll
    for (int s = 0; s < 8; ++s) {
      unsigned ch = 2 * s + hi;
      qf[s] = lds_read16(qs + r * 256 + 16 * (ch ^ (r & 15)));
    }
  }

  const float cs = 0.08838834764831845f * 1.4426950408889634f;
  float m = -__builtin_inff(), l = 0.f;
  f32x16 oacc[4];
#pragma unroll
  for (int c = 0; c < 4; ++c) oacc[c] = f32x16{};
  unsigned ka[8];
#pragma unroll
  for (int s = 0; s < 8; ++s) ka[s] = 256 * r + 16 * ((2 * s + hi) ^ fsw(r));
  unsigned va[4][2];
#pragma unroll
  for (int c = 0; c < 4; ++c)
#pragma unroll
    for (int t = 0; t < 2; ++t)
      va[c][t] = 16384 + 256 * (8 * t + 4 * hi + q4) + 16 * (4 * (c ^ q4) + (lo2 ^ (2 * t + hi))) + 8 * (p & 1);

  unsigned kslot = 8;
  for (int i = 0; i < 16; ++i) {
    BARRIER();
    unsigned kbase = lds0 + kslot * 16384;
    kslot += 2; if (kslot >= 10) kslot -= 10;
    f32x16 s0 = f32x16{}, s1 = f32x16{};
#pragma unroll
    for (int hf = 0; hf < 2; ++hf) {
      bf16x8 kf[4][2];
#pragma unroll
      for (int s = 0; s < 4; ++s) {
        kf[s][0] = lds_read16(kbase + ka[4 * hf + s]);
        kf[s][1] = lds_read16(kbase + ka[4 * hf + s] + 8192);
      }
#pragma unroll
      for (int s = 0; s < 4; ++s) {
        s0 = __builtin_amdgcn_mfma_f32_32x32x16_bf16(kf[s][0], qf[4 * hf + s], s0, 0, 0, 0);
        s1 = __builtin_amdgcn_mfma_f32_32x32x16_bf16(kf[s][1], qf[4 * hf + s], s1, 0, 0, 0);
      }
    }
    s16x4 vf[2][4][2];
#pragma unroll
    for (int ks = 0; ks < 2; ++ks)
#pragma unroll
      for (int c = 0; c < 4; ++c) {
        vf[ks][c][0] = tr_read(kbase + va[c][0] + ks * 4096);
        vf[ks][c][1] = tr_read(kbase + va[c][1] + ks * 4096);
      }
    float mx = s0[0];
#pragma unroll
    for (int j = 1; j < 16; ++j) mx = fmaxf(mx, s0[j]);
#pragma unroll
    for (int j = 0; j < 16; ++j) mx = fmaxf(mx, s1[j]);
    {
      auto rr = __builtin_amdgcn_permlane32_swap(__float_as_uint(mx), __float_as_uint(mx), false, false);
      mx = fmaxf(__uint_as_float(rr[0]), __uint_as_float(rr[1]));
    }
    float mnew = fmaxf(m, mx * cs);
    float alpha = __builtin_amdgcn_exp2f(m - mnew);
    m = mnew;
    if (__builtin_expect(__any(alpha != 1.f), 0)) {
#pragma unroll
      for (int c = 0; c < 4; ++c) oacc[c] *= alpha;
    }
    float ps = 0.f;
#pragma unroll
    for (int j = 0; j < 16; ++j) { s0[j] = __builtin_amdgcn_exp2f(fmaf(s0[j], cs, -mnew)); ps += s0[j]; }
    bf16x8 pb[2];
#pragma unroll
    for (int sp = 0; sp < 2; ++sp) {
      u32x4 u0 = {pk2(s0[8 * sp + 0], s0[8 * sp + 1]), pk2(s0[8 * sp + 2], s0[8 * sp + 3]),
                  pk2(s0[8 * sp + 4], s0[8 * sp + 5]), pk2(s0[8 * sp + 6], s0[8 * sp + 7])};
      pb[sp] = __builtin_bit_cast(bf16x8, u0);
    }
#pragma unroll
    for (int ks = 0; ks < 2; ++ks) {
#pragma unroll
      for (int c = 0; c < 4; ++c) {
        s16x4 a0 = vf[ks][c][0], a1 = vf[ks][c][1];
        bf16x8 a = {a0[0], a0[1], a0[2], a0[3], a1[0], a1[1], a1[2], a1[3]};
        oacc[c] = __builtin_amdgcn_mfma_f32_32x32x16_bf16(a, pb[ks], oacc[c], 0, 0, 0);
      }
    }
#pragma unroll
    for (int ks = 0; ks < 2; ++ks)
#pragma unroll
      for (int c = 0; c < 4; ++c) {
        vf[ks][c][0] = tr_read(kbase + va[c][0] + (2 + ks) * 4096);
        vf[ks][c][1] = tr_read(kbase + va[c][1] + (2 + ks) * 4096);
      }
#pragma unroll
    for (int j = 0; j < 16; ++j) { s1[j] = __builtin_amdgcn_exp2f(fmaf(s1[j], cs, -mnew)); ps += s1[j]; }
    l = l * alpha + ps;
#pragma unroll
    for (int sp = 0; sp < 2; ++sp) {
      u32x4 u1 = {pk2(s1[8 * sp + 0], s1[8 * sp + 1]), pk2(s1[8 * sp + 2], s1[8 * sp + 3]),
                  pk2(s1[8 * sp + 4], s1[8 * sp + 5]), pk2(s1[8 * sp + 6], s1[8 * sp + 7])};
      pb[sp] = __builtin_bit_cast(bf16x8, u1);
    }
#pragma unroll
    for (int ks = 0; ks < 2; ++ks) {
#pragma unroll
      for (int c = 0; c < 4; ++c) {
        s16x4 a0 = vf[ks][c][0], a1 = vf[ks][c][1];
        bf16x8 a = {a0[0], a0[1], a0[2], a0[3], a1[0], a1[1], a1[2], a1[3]};
        oacc[c] = __builtin_amdgcn_mfma_f32_32x32x16_bf16(a, pb[ks], oacc[c], 0, 0, 0);
      }
    }
  }
  {
    auto rr = __builtin_amdgcn_permlane32_swap(__float_as_uint(l), __float_as_uint(l), false, false);
    float lt = __uint_as_float(rr[0]) + __uint_as_float(rr[1]);
    float inv = 1.f / lt;
    if (qrow < SQ) {
      char* ob = (char*)OT + ((size_t)((7 * b + (qrow >> 4)) * 128 + 4 * h)) * 1024 + (qrow & 15) * 16 + 8 * hi;
#pragma unroll
      for (int c = 0; c < 4; ++c)
#pragma unroll
        for (int g = 0; g < 4; ++g) {
          u32x2 v = {pk2(oacc[c][4 * g + 0] * inv, oacc[c][4 * g + 1] * inv),
                     pk2(oacc[c][4 * g + 2] * inv, oacc[c][4 * g + 3] * inv)};
          *(u32x2*)(ob + c * 1024 + g * 256) = v;
        }
    }
  }
}

constexpr unsigned G2_LDS = 3 * 49152;
__global__ __launch_bounds__(384, 1) void oproj_kernel(const short* __restrict__ OT, const short* __restrict__ WT,
                                                       short* __restrict__ out) {
  extern __shared__ __attribute__((aligned(16))) char smem[];
  const unsigned lds0 = (unsigned)(uintptr_t)smem;
  const int tid = threadIdx.x;
  const int w = __builtin_amdgcn_readfirstlane(tid >> 6);
  const int lane = tid & 63;
  const int bid = blockIdx.x;
  const int mt = bid % 14, nt = bid / 14;
  if (w >= 4) {
    const int L = w - 4;
    const char* Ab = (const char*)OT + ((size_t)((mt * 4 + 2 * L) * 128)) * 1024 + lane * 16;
    const char* Bb = (const char*)WT + ((size_t)((nt * 2 + L) * 128)) * 1024 + lane * 16;
    auto stage = [&](int st, unsigned slot) {
      unsigned base = lds0 + slot * 49152;
#pragma unroll
      for (int k = 0; k < 8; ++k) {
#pragma unroll
        for (int mb = 0; mb < 2; ++mb)
          glds16<false>(Ab + (size_t)(mb * 128 + st * 8 + k) * 1024, base + (2 * L + mb) * 8192 + k * 1024);
        glds16<false>(Bb + (st * 8 + k) * 1024, base + 32768 + L * 8192 + k * 1024);
      }
    };
    stage(0, 0); stage(1, 1);
    unsigned slot = 2;
    for (int st = 0; st < 16; ++st) {
      if (st <= 14) WAITV(24);
      else WAITV(0);
      __builtin_amdgcn_s_barrier();
      if (st + 2 < 16) { stage(st + 2, slot); slot = slot + 1; if (slot >= 3) slot = 0; }
    }
    return;
  }
  f32x4 acc0 = f32x4{}, acc1 = f32x4{};
  unsigned slot = 0;
  for (int st = 0; st < 16; ++st) {
    BARRIER();
    unsigned base = lds0 + slot * 49152 + lane * 16;
#pragma unroll
    for (int hf = 0; hf < 2; ++hf) {
      bf16x8 a[4], b0[4], b1[4];
#pragma unroll
      for (int k = 0; k < 4; ++k) {
        a[k] = lds_read16(base + w * 8192 + (4 * hf + k) * 1024);
        b0[k] = lds_read16(base + 32768 + (4 * hf + k) * 1024);
        b1[k] = lds_read16(base + 32768 + 8192 + (4 * hf + k) * 1024);
      }
#pragma unroll
      for (int k = 0; k < 4; ++k) {
        acc0 = __builtin_amdgcn_mfma_f32_16x16x32_bf16(a[k], b0[k], acc0, 0, 0, 0);
        acc1 = __builtin_amdgcn_mfma_f32_16x16x32_bf16(a[k], b1[k], acc1, 0, 0, 0);
      }
    }
    slot = slot + 1; if (slot >= 3) slot = 0;
  }
  const int g = lane >> 4, li = lane & 15;
  const int m0 = mt * 64 + 16 * w + 4 * g, n0 = nt * 32 + li;
#pragma unroll
  for (int j = 0; j < 4; ++j) {
    unsigned v0 = pk2(acc0[j], 0.f), v1 = pk2(acc1[j], 0.f);
    out[(size_t)(m0 + j) * 512 + n0] = (short)(v0 & 0xffff);
    out[(size_t)(m0 + j) * 512 + n0 + 16] = (short)(v1 & 0xffff);
  }
}

extern "C" void kernel_launch(void* const* d_in, const int* in_sizes, int n_in, void* d_out, int out_size,
                              void* d_ws, size_t ws_size, hipStream_t stream) {
  const short* x = (const short*)d_in[0];
  const short* kc = (const short*)d_in[1];
  const short* vc = (const short*)d_in[2];
  const short* wq = (const short*)d_in[3];
  const short* wo = (const short*)d_in[4];
  short* OT = (short*)d_ws;
  short* WT = (short*)((char*)d_ws + (size_t)8 * 1024 * 1024);
  (void)hipFuncSetAttribute((const void*)attn_kernel, hipFuncAttributeMaxDynamicSharedMemorySize, LDS_BYTES);
  (void)hipFuncSetAttribute((const void*)oproj_kernel, hipFuncAttributeMaxDynamicSharedMemorySize, G2_LDS);
  attn_kernel<<<256, 384, LDS_BYTES, stream>>>(x, kc, vc, wq, wo, OT, WT);
  oproj_kernel<<<224, 384, G2_LDS, stream>>>(OT, WT, (short*)d_out);
}
```
